```python
import torch

def _softmax(x, axis=-1):
    return torch.softmax(x, dim=axis)
def _gelu(x):
    return torch.nn.functional.gelu(x, approximate="tanh")
def _silu(x):
    return torch.nn.functional.silu(x)
def _sigmoid(x):
    return torch.sigmoid(x)
def _layernorm(x, axis=-1, eps=1e-5):
    m = x.mean(dim=axis, keepdim=True)
    v = x.var(dim=axis, keepdim=True, unbiased=False)
    return (x - m) / torch.sqrt(v + eps)
def _scatter_add(tgt, idx, src, axis=0):
    return tgt.clone().index_add_(axis, idx, src)
def _segment_sum(src, seg, n):
    out = torch.zeros((n,) + src.shape[1:], dtype=src.dtype)
    return out.index_add_(0, seg, src)
def _pad(x, pw):
    flat = tuple(v for pair in reversed(pw) for v in pair)
    return torch.nn.functional.pad(x, flat)

kernel_name = 'recurrent_scan__gqa_block_cached__dense_activation_relu__decode'

def setup_inputs():

    _w = (lambda a: (lambda t: t.to(torch.bfloat16) if t.is_floating_point() else t)(torch.as_tensor(a)))
    def _A_setup():
        B, Sq, D = 2, 1, 512
        Hq, Hkv, Sk, Dh = 32, 8, 32768, 128
        assert Hq % Hkv == 0, (Hq, Hkv)
        return {
            "x": _w((lambda *s: torch.randn(*s, dtype=torch.bfloat16) * 0.05)(B, Sq, D)),
            "k_cache": _w((lambda *s: torch.randn(*s, dtype=torch.bfloat16) * 0.8)(B, Hkv, Sk, Dh)),
            "v_cache": _w((lambda *s: torch.randn(*s, dtype=torch.bfloat16) * 0.8)(B, Hkv, Sk, Dh)),
            "wq": _w((lambda *s: torch.randn(*s, dtype=torch.bfloat16) * 0.8)(D, Hq * Dh) * (D ** -0.5)),
            "wo": _w((lambda *s: torch.randn(*s, dtype=torch.bfloat16) * 0.8)(Hq * Dh, D) * ((Hq * Dh) ** -0.5)),
        }
    _A_in = _A_setup()

    def _B_setup():
        B, S, D, Dff = 2, 1, 512, 1792
        return {
            "x": _w((lambda *s: torch.randn(*s, dtype=torch.bfloat16) * 0.05)(B, S, D)),
            "w1": _w((lambda *s: torch.randn(*s, dtype=torch.bfloat16) * 0.2)(D, Dff)),
            "w2": _w((lambda *s: torch.randn(*s, dtype=torch.bfloat16) * 0.2)(Dff, D)),
        }
    _B_in = _B_setup()

    return {
        "A_x": _A_in["x"],
        "A_k_cache": _A_in["k_cache"],
        "A_v_cache": _A_in["v_cache"],
        "A_wq": _A_in["wq"],
        "A_wo": _A_in["wo"],
        "B_x": _B_in["x"],
        "B_w1": _B_in["w1"],
        "B_w2": _B_in["w2"],
    }

def reference(A_x, A_k_cache, A_v_cache, A_wq, A_wo, B_x, B_w1, B_w2):
    def _A_ref(x, k_cache, v_cache, wq, wo):
        B, Sq, D = x.shape
        Hq, Dh = 32, 128
        Hkv = k_cache.shape[1]
        rep = Hq // Hkv
        q = (lambda x, *d: x.permute(*d))((x @ wq).reshape(B, Sq, Hq, Dh), 0, 2, 1, 3)
        k = (lambda x, n, axis: torch.repeat_interleave(x, n, dim=axis))(k_cache, rep, axis=1)
        v = (lambda x, n, axis: torch.repeat_interleave(x, n, dim=axis))(v_cache, rep, axis=1)
        scores = torch.einsum("bhqd,bhkd->bhqk", q, k) * (Dh ** -0.5)
        p = _softmax(scores, axis=-1)
        o = torch.einsum("bhqk,bhkd->bhqd", p, v)
        return (lambda x, *d: x.permute(*d))(o, 0, 2, 1, 3).reshape(B, Sq, Hq * Dh) @ wo

    def _B_ref(x, w1, w2):
        h = torch.einsum("bsd,df->bsf", x, w1)
        zero = torch.zeros_like(h)
        h = torch.maximum(h, zero)
        return torch.einsum("bsf,fd->bsd", h, w2)

    x = A_x
    xn = _layernorm(x, axis=-1)
    s = _A_ref(xn, A_k_cache, A_v_cache, A_wq, A_wo)
    g = _silu(_B_ref(xn, B_w1, B_w2))
    return x + s * g
```

```cpp
#include <hip/hip_runtime.h>
#include <hip/hip_bf16.h>
#include <cstdint>

namespace {

typedef unsigned u32x4 __attribute__((ext_vector_type(4)));
typedef unsigned u32x2 __attribute__((ext_vector_type(2)));
typedef float f32x4 __attribute__((ext_vector_type(4)));
typedef float f32x2 __attribute__((ext_vector_type(2)));
typedef __bf16 bf16x2 __attribute__((ext_vector_type(2)));
typedef short s16x8 __attribute__((ext_vector_type(8)));
typedef short s16x4 __attribute__((ext_vector_type(4)));
typedef unsigned long long u64;
#define GAS __attribute__((address_space(1)))
typedef GAS u64 gu64;

constexpr int SK = 32768, DH = 128, DM = 512, DFF = 1792, HKV = 8;
constexpr int NSPLIT = 16, KEYS = SK / NSPLIT;
constexpr int NSW = 8;
constexpr int NTHR = (NSW + 1) * 64;
constexpr int NT = KEYS / 16 / NSW;
constexpr int W = 4;
constexpr int NFFN = DFF / 8;
static_assert(NT % W == 0, "ring");

constexpr unsigned WS_E = 0;
constexpr unsigned WS_FLAG = 4096;
constexpr unsigned WS_Q = 8192;
constexpr unsigned WS_Y = 0x20000;
constexpr unsigned WS_H1 = 0x200000;
constexpr unsigned WS_H2 = 0x400000;
constexpr unsigned WS_END = 0x500000;

constexpr float C2 = 0.08838834764831845f * 1.4426950408889634f;
constexpr u64 SPIN_TICKS = 4000000ull;

constexpr int L_O = 0;
constexpr int L_M = L_O + NSW * 2048;
constexpr int L_L = L_M + NSW * 16;
constexpr int L_Q = L_L + NSW * 16;
constexpr int L_H = L_Q + NSW * 128;
constexpr int L_QF = L_H + NSW * 64;
constexpr int L_OS = L_QF + 1024;
constexpr int L_TAG = L_OS + 128;
constexpr int L_END = L_TAG + 16;

__device__ __forceinline__ float asf(unsigned u) { return __builtin_bit_cast(float, u); }
__device__ __forceinline__ unsigned asu(float f) { return __builtin_bit_cast(unsigned, f); }
__device__ __forceinline__ float bf_lo(unsigned u) { return asf(u << 16); }
__device__ __forceinline__ float bf_hi(unsigned u) { return asf(u & 0xffff0000u); }
__device__ __forceinline__ unsigned cvtpk(float lo, float hi) {
  const f32x2 v = {lo, hi};
  return __builtin_bit_cast(unsigned, __builtin_convertvector(v, bf16x2));
}
__device__ __forceinline__ float rbf(float x) { return bf_lo(cvtpk(x, x)); }
template <int CTRL> __device__ __forceinline__ float dpp(float x) {
  return asf(__builtin_amdgcn_mov_dpp((int)asu(x), CTRL, 0xf, 0xf, true));
}
constexpr int XOR1 = 0xB1, XOR2 = 0x4E, HMIRR = 0x141, MIRR = 0x140, ROR4 = 0x124, ROR8 = 0x128;
__device__ __forceinline__ float red32(float a, float b) {
  auto r = __builtin_amdgcn_permlane32_swap(asu(a), asu(b), false, false);
  return asf((unsigned)r[0]) + asf((unsigned)r[1]);
}
__device__ __forceinline__ float red16(float a, float b) {
  auto r = __builtin_amdgcn_permlane16_swap(asu(a), asu(b), false, false);
  return asf((unsigned)r[0]) + asf((unsigned)r[1]);
}
__device__ __forceinline__ float max_x16_x32(float x) {
  auto s = __builtin_amdgcn_permlane16_swap(asu(x), asu(x), false, false);
  x = fmaxf(asf((unsigned)s[0]), asf((unsigned)s[1]));
  auto t = __builtin_amdgcn_permlane32_swap(asu(x), asu(x), false, false);
  return fmaxf(asf((unsigned)t[0]), asf((unsigned)t[1]));
}
__device__ __forceinline__ float row_sum16(float x) {
  x += dpp<XOR1>(x); x += dpp<XOR2>(x); x += dpp<HMIRR>(x); x += dpp<MIRR>(x); return x;
}
__device__ __forceinline__ float wave_sum(float x) { x = row_sum16(x); x = red16(x, x); return red32(x, x); }
__device__ __forceinline__ float wave_max(float x) {
  x = fmaxf(x, dpp<XOR1>(x)); x = fmaxf(x, dpp<XOR2>(x)); x = fmaxf(x, dpp<HMIRR>(x)); x = fmaxf(x, dpp<MIRR>(x));
  return max_x16_x32(x);
}
__device__ __forceinline__ u32x4 ldnt(const void* p) { return __builtin_nontemporal_load(reinterpret_cast<const u32x4*>(p)); }
__device__ __forceinline__ void st_g(unsigned char* ws, unsigned off, u64 v) {
  __hip_atomic_store((gu64*)(ws + off), v, __ATOMIC_RELAXED, __HIP_MEMORY_SCOPE_AGENT);
}
__device__ __forceinline__ u64 ld_g(unsigned char* ws, unsigned off) {
  return __hip_atomic_load((gu64*)(ws + off), __ATOMIC_RELAXED, __HIP_MEMORY_SCOPE_AGENT);
}
#define LD16(off) __builtin_amdgcn_raw_buffer_load_b128(rsrc, (off), 0, 16)
#define ST16(v, off) __builtin_amdgcn_raw_buffer_store_b128((v), rsrc, (off), 0, 16)
__device__ __forceinline__ bool timed_out(u64 t0) { return __builtin_amdgcn_s_memrealtime() - t0 > SPIN_TICKS; }
#define BARRIER() do { asm volatile("s_waitcnt lgkmcnt(0)" ::: "memory"); __builtin_amdgcn_s_barrier(); asm volatile("" ::: "memory"); } while (0)

__device__ __forceinline__ void ln_stats(u32x4 xv, float& mb, float& sd) {
  float e[8];
#pragma unroll
  for (int i = 0; i < 4; ++i) { e[2 * i] = bf_lo(xv[i]); e[2 * i + 1] = bf_hi(xv[i]); }
  float s = ((e[0] + e[1]) + (e[2] + e[3])) + ((e[4] + e[5]) + (e[6] + e[7]));
  const float mean = wave_sum(s) * (1.0f / DM);
  float q = 0.f;
#pragma unroll
  for (int i = 0; i < 8; ++i) { const float d = e[i] - mean; q = fmaf(d, d, q); }
  const float var = wave_sum(q) * (1.0f / DM);
  mb = rbf(mean);
  sd = rbf(sqrtf(rbf(rbf(var) + 1e-5f)));
}
__device__ __forceinline__ float ln_apply(unsigned short xr, float mb, float sd) {
  return rbf(rbf(bf_lo(xr) - mb) / sd);
}

struct Params {
  const unsigned short* x; const unsigned short* k; const unsigned short* v; const unsigned short* wq;
  const unsigned short* wo; const unsigned short* w1; const unsigned short* w2; unsigned short* out; unsigned char* ws;
};

__global__ void __launch_bounds__(NTHR, 1) fused_decode(Params p) {
  __shared__ __attribute__((aligned(16))) unsigned char lds[L_END];
  const int tid = threadIdx.x, lane = tid & 63;
  const int wave = __builtin_amdgcn_readfirstlane(tid >> 6);
  const int bid = blockIdx.x, kvh = bid & 7, r = bid >> 3, b = r >> 4, sp = r & 15;
  const bool has_ffn = r < 28;
  const int fi = has_ffn ? kvh * 28 + r : 0;
  unsigned char* ws = p.ws;
  const auto rsrc = __builtin_amdgcn_make_buffer_rsrc(ws, 0, WS_END, 0x00020000);
  const int n = lane & 15, g = lane >> 4;
  const int cg = kvh * 16 + sp;
  u32x4 wov[4];

  if (wave < NSW) {
    const int r0 = 64 * wave + (lane >> 1), r1 = r0 + 32, rk = 64 * wave + lane;
    const u32x4 xa = *reinterpret_cast<const u32x4*>(p.x + 8 * lane);
    const u32x4 xb = *reinterpret_cast<const u32x4*>(p.x + DM + 8 * lane);
    const unsigned short x00 = p.x[r0], x01 = p.x[r1], x0k = p.x[rk];
    const unsigned short x10 = p.x[DM + r0], x11 = p.x[DM + r1], x1k = p.x[DM + rk];
    const int qcol = kvh * 512 + r * 16 + 8 * (lane & 1);
    const u32x4 wq0 = ldnt(p.wq + (size_t)r0 * 4096 + qcol);
    const u32x4 wq1 = ldnt(p.wq + (size_t)r1 * 4096 + qcol);
    const u32x4 w1v = ldnt(p.w1 + (size_t)rk * DFF + 8 * fi);
    __builtin_amdgcn_sched_barrier(0);

    const size_t kvbase = ((size_t)(b * HKV + kvh) * SK + (size_t)sp * KEYS + (size_t)wave * 16) * DH;
    const unsigned short* kp = p.k + kvbase + (size_t)(n >> 2) * DH + 8 * (4 * g + (n & 3));
    const unsigned short* vp = p.v + kvbase + 8 * lane;
    u32x4 kr[W][4], vr[W][4];
#define ISSUE(slot, t) do { _Pragma("unroll") for (int i_ = 0; i_ < 4; ++i_) kr[slot][i_] = ldnt(kp + (size_t)(t) * (NSW * 16 * DH) + i_ * 4 * DH); \
                            _Pragma("unroll") for (int i_ = 0; i_ < 4; ++i_) vr[slot][i_] = ldnt(vp + (size_t)(t) * (NSW * 16 * DH) + i_ * 4 * DH); \
                            __builtin_amdgcn_sched_barrier(0); } while (0)
#define PACE() do { asm volatile("s_waitcnt vmcnt(8)" ::: "memory"); __builtin_amdgcn_sched_barrier(0); } while (0)
    ISSUE(0, 0);
    ISSUE(1, 1);

    {
      float m0, s0, m1, s1;
      ln_stats(xa, m0, s0); ln_stats(xb, m1, s1);
      const float a00 = ln_apply(x00, m0, s0), a01 = ln_apply(x01, m0, s0), a0k = ln_apply(x0k, m0, s0);
      const float a10 = ln_apply(x10, m1, s1), a11 = ln_apply(x11, m1, s1), a1k = ln_apply(x1k, m1, s1);
      float qa[16], ha[16];
#pragma unroll
      for (int i = 0; i < 4; ++i) {
        const float u0 = bf_lo(wq0[i]), u1 = bf_hi(wq0[i]), v0 = bf_lo(wq1[i]), v1 = bf_hi(wq1[i]);
        qa[2 * i] = fmaf(a00, u0, a01 * v0); qa[2 * i + 1] = fmaf(a00, u1, a01 * v1);
        qa[8 + 2 * i] = fmaf(a10, u0, a11 * v0); qa[8 + 2 * i + 1] = fmaf(a10, u1, a11 * v1);
        const float z0 = bf_lo(w1v[i]), z1 = bf_hi(w1v[i]);
        ha[2 * i] = a0k * z0; ha[2 * i + 1] = a0k * z1; ha[8 + 2 * i] = a1k * z0; ha[8 + 2 * i + 1] = a1k * z1;
      }
#pragma unroll
      for (int i = 0; i < 8; ++i) { qa[i] = red32(qa[i], qa[i + 8]); ha[i] = red32(ha[i], ha[i + 8]); }
#pragma unroll
      for (int i = 0; i < 4; ++i) { qa[i] = red16(qa[i], qa[i + 4]); ha[i] = red16(ha[i], ha[i + 4]); }
#pragma unroll
      for (int i = 0; i < 4; ++i) {
        float t = qa[i]; t += dpp<ROR8>(t); t += dpp<ROR4>(t); t += dpp<XOR2>(t); qa[i] = t;
        ha[i] = row_sum16(ha[i]);
      }
      const int bb = lane >> 5, ch = (lane >> 4) & 1;
      if ((lane & 14) == 0)
        *reinterpret_cast<f32x4*>(lds + L_Q + wave * 128 + bb * 64 + (8 * (lane & 1) + 4 * ch) * 4) = f32x4{qa[0], qa[1], qa[2], qa[3]};
      if ((lane & 15) == 0)
        *reinterpret_cast<f32x4*>(lds + L_H + wave * 64 + bb * 32 + ch * 16) = f32x4{ha[0], ha[1], ha[2], ha[3]};
    }
    BARRIER();
#pragma unroll
    for (int t = 2; t < W; ++t) { PACE(); ISSUE(t, t); }
    BARRIER();
    const s16x8 qf = *reinterpret_cast<const s16x8*>(lds + L_QF + ((n >> 2) * 16 + 4 * g + (n & 3)) * 16);
    const unsigned jm = n & 3;
    const unsigned mask0 = jm == 0 ? 0x0000ffffu : (jm == 1 ? 0xffff0000u : 0u);
    const unsigned mask1 = jm == 2 ? 0x0000ffffu : (jm == 3 ? 0xffff0000u : 0u);
    const bool sel1 = (lane & 1) != 0, sel2 = (lane & 2) != 0;
    float m = -INFINITY, mc = 0.f, l = 0.f;
    f32x4 o0 = {0.f, 0.f, 0.f, 0.f}, o1 = {0.f, 0.f, 0.f, 0.f};

#define TILE(slot) do { \
      float s_[4]; \
      _Pragma("unroll") for (int i_ = 0; i_ < 4; ++i_) { \
        const f32x4 d_ = __builtin_amdgcn_mfma_f32_16x16x32_bf16(__builtin_bit_cast(s16x8, kr[slot][i_]), qf, f32x4{0.f, 0.f, 0.f, 0.f}, 0, 0, 0); \
        const float t0_ = sel1 ? d_[1] : d_[0], t1_ = sel1 ? d_[3] : d_[2]; \
        float u_ = sel2 ? t1_ : t0_; u_ += dpp<XOR1>(u_); u_ += dpp<XOR2>(u_); s_[i_] = u_; } \
      const float mx_ = fmaxf(fmaxf(s_[0], s_[1]), fmaxf(s_[2], s_[3])); \
      if (__builtin_amdgcn_ballot_w64(mx_ > m) != 0) { \
        const float mn_ = fmaxf(m, max_x16_x32(mx_)); \
        const float mcn_ = mn_ * C2; \
        const float al_ = (m == -INFINITY) ? 0.f : __builtin_amdgcn_exp2f(mc - mcn_); \
        l *= al_; m = mn_; mc = mcn_; \
        const float a0_ = asf(__builtin_amdgcn_readlane((int)asu(al_), 0)); \
        const float a1_ = asf(__builtin_amdgcn_readlane((int)asu(al_), 4)); \
        const float a2_ = asf(__builtin_amdgcn_readlane((int)asu(al_), 8)); \
        const float a3_ = asf(__builtin_amdgcn_readlane((int)asu(al_), 12)); \
        const float ao_ = g == 0 ? a0_ : (g == 1 ? a1_ : (g == 2 ? a2_ : a3_)); \
        o0 *= ao_; o1 *= ao_; } \
      _Pragma("unroll") for (int i_ = 0; i_ < 4; ++i_) { \
        const float p_ = __builtin_amdgcn_exp2f(fmaf(s_[i_], C2, -mc)); \
        l += p_; \
        const unsigned pb_ = cvtpk(p_, p_); \
        const u32x2 a_ = {pb_ & mask0, pb_ & mask1}; \
        const u32x2 va_ = {vr[slot][i_][0], vr[slot][i_][1]}, vb_ = {vr[slot][i_][2], vr[slot][i_][3]}; \
        o0 = __builtin_amdgcn_mfma_f32_16x16x16bf16_1k(__builtin_bit_cast(s16x4, a_), __builtin_bit_cast(s16x4, va_), o0, 0, 0, 0); \
        o1 = __builtin_amdgcn_mfma_f32_16x16x16bf16_1k(__builtin_bit_cast(s16x4, a_), __builtin_bit_cast(s16x4, vb_), o1, 0, 0, 0); } \
      asm volatile("" : "+v"(o0), "+v"(o1)); \
    } while (0)

#pragma unroll 1
    for (int t0 = 0; t0 < NT - W; t0 += W) {
#pragma unroll
      for (int s = 0; s < W; ++s) { TILE(s); __builtin_amdgcn_sched_barrier(0); PACE(); ISSUE(s, t0 + s + W); }
    }
    TILE(0);
    __builtin_amdgcn_sched_barrier(0);
    {
      const unsigned short* wp = p.wo + (size_t)(kvh * 512 + 32 * sp + (lane >> 3)) * DM + 64 * wave + 8 * (lane & 7);
#pragma unroll
      for (int i = 0; i < 4; ++i) wov[i] = ldnt(wp + (size_t)i * 8 * DM);
      __builtin_amdgcn_sched_barrier(0);
    }
#pragma unroll
    for (int s = 1; s < W; ++s) { TILE(s); }
    l = red16(l, l); l = red32(l, l);
    *reinterpret_cast<f32x4*>(lds + L_O + wave * 2048 + g * 512 + n * 32) = o0;
    *reinterpret_cast<f32x4*>(lds + L_O + wave * 2048 + g * 512 + n * 32 + 16) = o1;
    if ((lane & 0x33) == 0) {
      *reinterpret_cast<float*>(lds + L_M + wave * 16 + (n >> 2) * 4) = mc;
      *reinterpret_cast<float*>(lds + L_L + wave * 16 + (n >> 2) * 4) = l;
    }
    BARRIER();
    const unsigned tag = *reinterpret_cast<const unsigned*>(lds + L_TAG);
    {
      const int h = tid >> 7, d = tid & 127;
      float mw[NSW], M = -INFINITY;
#pragma unroll
      for (int w = 0; w < NSW; ++w) { mw[w] = *reinterpret_cast<const float*>(lds + L_M + w * 16 + h * 4); M = fmaxf(M, mw[w]); }
      float O = 0.f, L = 0.f;
#pragma unroll
      for (int w = 0; w < NSW; ++w) {
        const float wt = __builtin_amdgcn_exp2f(mw[w] - M);
        O = fmaf(wt, *reinterpret_cast<const float*>(lds + L_O + w * 2048 + h * 512 + d * 4), O);
        L = fmaf(wt, *reinterpret_cast<const float*>(lds + L_L + w * 16 + h * 4), L);
      }
      const int j = h * 4 + (d >> 5);
      const unsigned dst = WS_H1 + (unsigned)(kvh + 8 * (b * 16 + j)) * 8192u;
      st_g(ws, dst + sp * 256 + (d & 31) * 8, ((u64)tag << 32) | asu(O));
      if ((d & 31) == 0) {
        const u32x4 ml = {asu(M), tag, asu(L), tag};
        ST16(ml, dst + 4096 + sp * 16);
      }
    }
    BARRIER();
    {
      float acc[8];
#pragma unroll
      for (int c = 0; c < 8; ++c) acc[c] = 0.f;
#pragma unroll
      for (int i = 0; i < 4; ++i) {
        const float ov = *reinterpret_cast<const float*>(lds + L_OS + (8 * i + (lane >> 3)) * 4);
#pragma unroll
        for (int c = 0; c < 4; ++c) { acc[2 * c] = fmaf(ov, bf_lo(wov[i][c]), acc[2 * c]); acc[2 * c + 1] = fmaf(ov, bf_hi(wov[i][c]), acc[2 * c + 1]); }
      }
#pragma unroll
      for (int c = 0; c < 4; ++c) acc[c] = red32(acc[c], acc[c + 4]);
#pragma unroll
      for (int c = 0; c < 2; ++c) acc[c] = red16(acc[c], acc[c + 2]);
      const float e0 = acc[0] + dpp<ROR8>(acc[0]), e1 = acc[1] + dpp<ROR8>(acc[1]);
      const float sv = (lane & 8) ? e1 : e0;
      const int col = 64 * wave + 8 * (lane & 7) + 4 * (lane >> 5) + 2 * ((lane >> 4) & 1) + ((lane >> 3) & 1);
      st_g(ws, WS_H2 + (unsigned)(b * 128 + (col >> 2)) * 4096u + (kvh * 16 + sp) * 32 + (col & 3) * 8,
           ((u64)tag << 32) | asu(sv));
    }
#undef TILE
#undef ISSUE
#undef PACE
  } else {
    __builtin_amdgcn_s_setprio(3);
    const u64 ep = ld_g(ws, WS_E) + 1ull;
    u32x4 w2v[8];
#pragma unroll
    for (int i = 0; i < 8; ++i) w2v[i] = ldnt(p.w2 + (size_t)(8 * fi + i) * DM + 8 * lane);
    const unsigned xo = *reinterpret_cast<const unsigned*>(p.x + b * DM + 4 * cg + 2 * (lane & 1));
    const unsigned tag = (unsigned)ep;
    {
      const u32x4 z = {0u, ~tag, 0u, ~tag};
#pragma unroll
      for (int i = 0; i < 4; ++i) { ST16(z, WS_H1 + bid * 8192 + i * 1024 + lane * 16); ST16(z, WS_H2 + (b * 128 + cg) * 4096 + i * 1024 + lane * 16); }
      if (lane < 16) ST16(z, WS_H1 + bid * 8192 + 4096 + lane * 16);
    }
    BARRIER();
    float hs = 0.f;
    {
      float qs = 0.f;
      const int qi = lane & 31, hi_ = lane & 15;
#pragma unroll
      for (int w = 0; w < NSW; ++w) {
        qs += *reinterpret_cast<const float*>(lds + L_Q + w * 128 + qi * 4);
        hs += *reinterpret_cast<const float*>(lds + L_H + w * 64 + hi_ * 4);
      }
      if (lane < 32)
        st_g(ws, WS_Q + (((lane >> 4) * 8 + kvh) * 512 + r * 16 + (lane & 15)) * 8, (ep << 16) | (cvtpk(qs, qs) & 0xffffu));
      hs = fmaxf(rbf(hs), 0.f);
    }
    if (has_ffn) {
      float y0[8], y1[8];
#pragma unroll
      for (int c = 0; c < 8; ++c) { y0[c] = 0.f; y1[c] = 0.f; }
#pragma unroll
      for (int i = 0; i < 8; ++i) {
        const float h0 = asf(__builtin_amdgcn_readlane((int)asu(hs), i));
        const float h1 = asf(__builtin_amdgcn_readlane((int)asu(hs), 8 + i));
#pragma unroll
        for (int c = 0; c < 4; ++c) {
          const float lo = bf_lo(w2v[i][c]), hi = bf_hi(w2v[i][c]);
          y0[2 * c] = fmaf(h0, lo, y0[2 * c]); y0[2 * c + 1] = fmaf(h0, hi, y0[2 * c + 1]);
          y1[2 * c] = fmaf(h1, lo, y1[2 * c]); y1[2 * c + 1] = fmaf(h1, hi, y1[2 * c + 1]);
        }
      }
      const unsigned yb = WS_Y + (2 * lane) * 4096 + fi * 16;
      ST16((u32x4{asu(y0[0]), asu(y0[1]), asu(y0[2]), asu(y0[3])}), yb);
      ST16((u32x4{asu(y0[4]), asu(y0[5]), asu(y0[6]), asu(y0[7])}), yb + 4096);
      ST16((u32x4{asu(y1[0]), asu(y1[1]), asu(y1[2]), asu(y1[3])}), yb + 128 * 4096);
      ST16((u32x4{asu(y1[4]), asu(y1[5]), asu(y1[6]), asu(y1[7])}), yb + 129 * 4096);
    }
    {
      const unsigned qb = WS_Q + (b * 8 + kvh) * 4096 + lane * 64;
      const unsigned t_hi = (unsigned)(ep >> 16), t_lo = ((unsigned)ep) << 16;
      const u64 t0 = __builtin_amdgcn_s_memrealtime();
      u32x4 qv[4];
      for (;;) {
        asm volatile("" ::: "memory");
#pragma unroll
        for (int i = 0; i < 4; ++i) qv[i] = LD16(qb + i * 16);
        unsigned bad = 0;
#pragma unroll
        for (int i = 0; i < 4; ++i)
          bad |= (qv[i][1] ^ t_hi) | (qv[i][3] ^ t_hi) | ((qv[i][0] ^ t_lo) & 0xffff0000u) | ((qv[i][2] ^ t_lo) & 0xffff0000u);
        if (__builtin_amdgcn_ballot_w64(bad != 0) == 0 || timed_out(t0)) break;
        __builtin_amdgcn_s_sleep(1);
      }
      u32x4 f;
#pragma unroll
      for (int i = 0; i < 4; ++i) f[i] = (qv[i][0] & 0xffffu) | (qv[i][2] << 16);
      *reinterpret_cast<u32x4*>(lds + L_QF + lane * 16) = f;
      if (lane == 0) *reinterpret_cast<unsigned*>(lds + L_TAG) = tag;
    }
    asm volatile("s_waitcnt vmcnt(0)" ::: "memory");
    if (lane == 0) st_g(ws, WS_FLAG + bid * 8, ep);
    BARRIER();
    {
      const unsigned e_lo = (unsigned)ep, e_hi = (unsigned)(ep >> 32);
      const u64 t0 = __builtin_amdgcn_s_memrealtime();
      for (;;) {
        asm volatile("" ::: "memory");
        const u32x4 f0 = LD16(WS_FLAG + lane * 16), f1 = LD16(WS_FLAG + 1024 + lane * 16);
        const unsigned bad = (f0[0] ^ e_lo) | (f0[1] ^ e_hi) | (f0[2] ^ e_lo) | (f0[3] ^ e_hi) | (f1[0] ^ e_lo) | (f1[1] ^ e_hi) | (f1[2] ^ e_lo) | (f1[3] ^ e_hi);
        if (__builtin_amdgcn_ballot_w64(bad != 0) == 0 || timed_out(t0)) break;
        __builtin_amdgcn_s_sleep(8);
      }
      asm volatile("" ::: "memory");
    }
    if (bid == 0 && lane == 0) st_g(ws, WS_E, ep);
    float gate0, gate1;
    {
      const unsigned yb = WS_Y + (b * 128 + cg) * 4096 + lane * 16;
      const u32x4 y0 = LD16(yb), y1 = LD16(yb + 1024), y2 = LD16(yb + 2048), y3 = LD16(yb + 3072);
      float ys[4];
#pragma unroll
      for (int c = 0; c < 4; ++c) {
        const float t3 = lane < 32 ? asf(y3[c]) : 0.f;
        ys[c] = wave_sum((asf(y0[c]) + asf(y1[c])) + (asf(y2[c]) + t3));
      }
      const float ya = rbf((lane & 1) ? ys[2] : ys[0]), yb2 = rbf((lane & 1) ? ys[3] : ys[1]);
      gate0 = rbf(ya / (1.f + __expf(-ya))); gate1 = rbf(yb2 / (1.f + __expf(-yb2)));
    }
    BARRIER();
    {
      const unsigned ib = WS_H1 + bid * 8192;
      const u64 t0 = __builtin_amdgcn_s_memrealtime();
      u32x4 ov[4], mv[4];
      for (;;) {
        asm volatile("" ::: "memory");
#pragma unroll
        for (int i = 0; i < 4; ++i) { ov[i] = LD16(ib + (4 * i + g) * 256 + n * 16); mv[i] = LD16(ib + 4096 + (4 * i + g) * 16); }
        unsigned bad = 0;
#pragma unroll
        for (int i = 0; i < 4; ++i) bad |= (ov[i][1] ^ tag) | (ov[i][3] ^ tag) | (mv[i][1] ^ tag) | (mv[i][3] ^ tag);
        if (__builtin_amdgcn_ballot_w64(bad != 0) == 0 || timed_out(t0)) break;
      }
      float M = -INFINITY;
#pragma unroll
      for (int i = 0; i < 4; ++i) M = fmaxf(M, asf(mv[i][0]));
      M = max_x16_x32(M);
      float L = 0.f, oa = 0.f, ob = 0.f;
#pragma unroll
      for (int i = 0; i < 4; ++i) {
        const float wt = __builtin_amdgcn_exp2f(asf(mv[i][0]) - M);
        L = fmaf(wt, asf(mv[i][2]), L);
        oa = fmaf(wt, asf(ov[i][0]), oa);
        ob = fmaf(wt, asf(ov[i][2]), ob);
      }
      L = red16(L, L); L = red32(L, L);
      oa = red16(oa, ob); oa = red32(oa, oa);
      if (lane < 32) *reinterpret_cast<float*>(lds + L_OS + (2 * n + g) * 4) = rbf(oa / L);
    }
    BARRIER();
    {
      const unsigned ib = WS_H2 + (b * 128 + cg) * 4096 + lane * 16;
      const u64 t0 = __builtin_amdgcn_s_memrealtime();
      u32x4 sv[4];
      for (;;) {
        asm volatile("" ::: "memory");
#pragma unroll
        for (int i = 0; i < 4; ++i) sv[i] = LD16(ib + i * 1024);
        unsigned bad = 0;
#pragma unroll
        for (int i = 0; i < 4; ++i) bad |= (sv[i][1] ^ tag) | (sv[i][3] ^ tag);
        if (__builtin_amdgcn_ballot_w64(bad != 0) == 0 || timed_out(t0)) break;
      }
      float s0 = (asf(sv[0][0]) + asf(sv[1][0])) + (asf(sv[2][0]) + asf(sv[3][0]));
      float s1 = (asf(sv[0][2]) + asf(sv[1][2])) + (asf(sv[2][2]) + asf(sv[3][2]));
      s0 += dpp<ROR8>(s0); s0 += dpp<ROR4>(s0); s0 += dpp<XOR2>(s0); s0 = red16(s0, s0); s0 = red32(s0, s0);
      s1 += dpp<ROR8>(s1); s1 += dpp<ROR4>(s1); s1 += dpp<XOR2>(s1); s1 = red16(s1, s1); s1 = red32(s1, s1);
      const float r0 = bf_lo(xo) + rbf(rbf(s0) * gate0), r1 = bf_hi(xo) + rbf(rbf(s1) * gate1);
      if (lane < 2) *reinterpret_cast<unsigned*>(p.out + b * DM + 4 * cg + 2 * lane) = cvtpk(r0, r1);
    }
  }
}

}

extern "C" void kernel_launch(void* const* d_in, const int* in_sizes, int n_in, void* d_out, int out_size, void* d_ws,
                              size_t ws_size, hipStream_t stream) {
  Params p;
  p.x = (const unsigned short*)d_in[0]; p.k = (const unsigned short*)d_in[1]; p.v = (const unsigned short*)d_in[2];
  p.wq = (const unsigned short*)d_in[3]; p.wo = (const unsigned short*)d_in[4];
  p.w1 = (const unsigned short*)d_in[6]; p.w2 = (const unsigned short*)d_in[7];
  p.out = (unsigned short*)d_out; p.ws = (unsigned char*)d_ws;
  hipLaunchKernelGGL(fused_decode, dim3(256), dim3(NTHR), 0, stream, p);
}
```

```cpp
#include <hip/hip_runtime.h>
#include <hip/hip_bf16.h>
#include <cstdint>

namespace {

typedef unsigned u32x4 __attribute__((ext_vector_type(4)));
typedef unsigned u32x2 __attribute__((ext_vector_type(2)));
typedef float f32x4 __attribute__((ext_vector_type(4)));
typedef float f32x2 __attribute__((ext_vector_type(2)));
typedef __bf16 bf16x2 __attribute__((ext_vector_type(2)));
typedef short s16x8 __attribute__((ext_vector_type(8)));
typedef short s16x4 __attribute__((ext_vector_type(4)));
typedef unsigned long long u64;
#define GAS __attribute__((address_space(1)))
typedef GAS u64 gu64;

constexpr int SK = 32768, DH = 128, DM = 512, DFF = 1792, HKV = 8;
constexpr int NSPLIT = 16, KEYS = SK / NSPLIT;
constexpr int NSW = 8;
#define NQW 4
#define INFL_PRO 3
#define INFL_MAIN 4
constexpr int NTHR = (NSW + NQW) * 64;
constexpr int NT = KEYS / 16 / NSW;
constexpr int W = 4;
constexpr int QR = DM / NQW / 32;
static_assert(NT % W == 0, "ring");

#define WS_BASE 0
constexpr unsigned WS_E = 0;
constexpr unsigned WS_Q = 0x2000;
constexpr unsigned WS_HX = 0x12000;
constexpr unsigned WS_YX = 0x20000;
constexpr unsigned WS_H1 = 0x200000;
constexpr unsigned WS_H2 = 0x400000;
constexpr unsigned WS_END = 0x500000;

constexpr float C2 = 0.08838834764831845f * 1.4426950408889634f;
constexpr u64 SPIN_TICKS = 4000000ull;

constexpr int L_O = 0;
constexpr int L_M = L_O + NSW * 2048;
constexpr int L_L = L_M + NSW * 16;
constexpr int L_Q = L_L + NSW * 16;
constexpr int L_XN = L_Q + 4 * 128;
constexpr int L_HS = L_XN + 4096;
constexpr int L_QF = L_HS + 2048;
constexpr int L_OS = L_QF + 1024;
constexpr int L_TAG = L_OS + 128;
constexpr int L_QCNT = L_TAG + 8;
constexpr int L_QRDY = L_TAG + 12;
constexpr int L_PROG = L_TAG + 16;
constexpr int L_END = L_TAG + 32;

__device__ __forceinline__ float asf(unsigned u) { return __builtin_bit_cast(float, u); }
__device__ __forceinline__ unsigned asu(float f) { return __builtin_bit_cast(unsigned, f); }
__device__ __forceinline__ float bf_lo(unsigned u) { return asf(u << 16); }
__device__ __forceinline__ float bf_hi(unsigned u) { return asf(u & 0xffff0000u); }
__device__ __forceinline__ unsigned cvtpk(float lo, float hi) {
  const f32x2 v = {lo, hi};
  return __builtin_bit_cast(unsigned, __builtin_convertvector(v, bf16x2));
}
__device__ __forceinline__ float rbf(float x) { return bf_lo(cvtpk(x, x)); }
template <int CTRL> __device__ __forceinline__ float dpp(float x) {
  return asf(__builtin_amdgcn_mov_dpp((int)asu(x), CTRL, 0xf, 0xf, true));
}
constexpr int XOR1 = 0xB1, XOR2 = 0x4E, HMIRR = 0x141, MIRR = 0x140, ROR4 = 0x124, ROR8 = 0x128;
__device__ __forceinline__ float red32(float a, float b) {
  auto r = __builtin_amdgcn_permlane32_swap(asu(a), asu(b), false, false);
  return asf((unsigned)r[0]) + asf((unsigned)r[1]);
}
__device__ __forceinline__ float red16(float a, float b) {
  auto r = __builtin_amdgcn_permlane16_swap(asu(a), asu(b), false, false);
  return asf((unsigned)r[0]) + asf((unsigned)r[1]);
}
__device__ __forceinline__ float max_x16_x32(float x) {
  auto s = __builtin_amdgcn_permlane16_swap(asu(x), asu(x), false, false);
  x = fmaxf(asf((unsigned)s[0]), asf((unsigned)s[1]));
  auto t = __builtin_amdgcn_permlane32_swap(asu(x), asu(x), false, false);
  return fmaxf(asf((unsigned)t[0]), asf((unsigned)t[1]));
}
__device__ __forceinline__ float row_sum16(float x) {
  x += dpp<XOR1>(x); x += dpp<XOR2>(x); x += dpp<HMIRR>(x); x += dpp<MIRR>(x); return x;
}
__device__ __forceinline__ float row_sum8_odd_even(float t) {
  t += dpp<ROR8>(t); t += dpp<ROR4>(t); t += dpp<XOR2>(t); return t;
}
__device__ __forceinline__ float wave_sum(float x) { x = row_sum16(x); x = red16(x, x); return red32(x, x); }
__device__ __forceinline__ u32x4 ldnt(const void* p) { return __builtin_nontemporal_load(reinterpret_cast<const u32x4*>(p)); }
__device__ __forceinline__ u32x4 ldw(const void* p) {
  return __builtin_nontemporal_load(reinterpret_cast<const u32x4*>(p));
}
__device__ __forceinline__ u32x4 ldf(const void* p) {
  return ldw(p);
}
__device__ __forceinline__ void st_g(unsigned char* ws, unsigned off, u64 v) {
  __hip_atomic_store((gu64*)(ws + off), v, __ATOMIC_RELAXED, __HIP_MEMORY_SCOPE_AGENT);
}
__device__ __forceinline__ u64 ld_g(unsigned char* ws, unsigned off) {
  return __hip_atomic_load((gu64*)(ws + off), __ATOMIC_RELAXED, __HIP_MEMORY_SCOPE_AGENT);
}
__device__ __forceinline__ unsigned f24(float x, unsigned tlo) { const unsigned u = asu(x); return ((u + 0x7fu + ((u >> 8) & 1u)) >> 8) | tlo; }
__device__ __forceinline__ float uf24(unsigned lo) { return asf(lo << 8); }
__device__ __forceinline__ u64 gran24(float x, unsigned tlo, unsigned thi) { return ((u64)thi << 32) | f24(x, tlo); }
#define BAD24(v) (((v)[1] ^ thi) | ((v)[3] ^ thi) | (((v)[0] ^ tlo) >> 24) | (((v)[2] ^ tlo) >> 24))
#define BAD16(v) (((v)[1] ^ qhi) | ((v)[3] ^ qhi) | (((v)[0] ^ qlo) >> 16) | (((v)[2] ^ qlo) >> 16))
#define LD16(off) __builtin_amdgcn_raw_buffer_load_b128(rsrc, (off), 0, 16)
#define ST16(v, off) __builtin_amdgcn_raw_buffer_store_b128((v), rsrc, (off), 0, 16)
__device__ __forceinline__ bool timed_out(u64 t0) { return __builtin_amdgcn_s_memrealtime() - t0 > SPIN_TICKS; }
#define STAMP(i) do {} while (0)
#define BARRIER() do { asm volatile("s_waitcnt lgkmcnt(0)" ::: "memory"); __builtin_amdgcn_s_barrier(); asm volatile("" ::: "memory"); } while (0)
typedef __attribute__((address_space(3))) unsigned lds_u32;
#define LDS_LD(off) __hip_atomic_load((lds_u32*)(lds + (off)), __ATOMIC_RELAXED, __HIP_MEMORY_SCOPE_WORKGROUP)
#define LDS_ST(off, v) __hip_atomic_store((lds_u32*)(lds + (off)), (v), __ATOMIC_RELAXED, __HIP_MEMORY_SCOPE_WORKGROUP)

__device__ __forceinline__ void ln_stats(u32x4 xv, float& mb, float& sd) {
  float e[8];
#pragma unroll
  for (int i = 0; i < 4; ++i) { e[2 * i] = bf_lo(xv[i]); e[2 * i + 1] = bf_hi(xv[i]); }
  float s = ((e[0] + e[1]) + (e[2] + e[3])) + ((e[4] + e[5]) + (e[6] + e[7]));
  const float mean = wave_sum(s) * (1.0f / DM);
  float q = 0.f;
#pragma unroll
  for (int i = 0; i < 8; ++i) { const float d = e[i] - mean; q = fmaf(d, d, q); }
  const float var = wave_sum(q) * (1.0f / DM);
  mb = rbf(mean);
  sd = rbf(sqrtf(rbf(rbf(var) + 1e-5f)));
}
__device__ __forceinline__ float ln_apply(float x, float mb, float sd) { return rbf(rbf(x - mb) / sd); }

struct Params {
  const unsigned short* x; const unsigned short* k; const unsigned short* v; const unsigned short* wq;
  const unsigned short* wo; const unsigned short* w1; const unsigned short* w2; unsigned short* out; unsigned char* ws;
};

__global__ void __launch_bounds__(NTHR, 1) fused_decode(Params p) {
  __shared__ __attribute__((aligned(16))) unsigned char lds[L_END];
  const int tid = threadIdx.x, lane = tid & 63;
  const int wave = __builtin_amdgcn_readfirstlane(tid >> 6);
  const int bid = blockIdx.x, kvh = bid & 7, r = bid >> 3, b = r >> 4, sp = r & 15;
  unsigned char* ws = p.ws;
  const auto rsrc = __builtin_amdgcn_make_buffer_rsrc(ws, 0, WS_END, 0x00020000);
  const int n = lane & 15, g = lane >> 4;
  const int cg = kvh * 16 + sp;

  if (wave < NSW) {
    u64 st0 = 0, st1 = 0, st2 = 0, st3 = 0;
    const size_t kvbase = ((size_t)(b * HKV + kvh) * SK + (size_t)sp * KEYS + (size_t)wave * 16) * DH;
    const unsigned short* kp = p.k + kvbase + (size_t)(n >> 2) * DH + 8 * (4 * g + (n & 3));
    const unsigned short* vp = p.v + kvbase + 8 * lane;
    const int rot = (bid * 5 + (bid >> 3) * 11) & (NT - 1);
    u32x4 kr[W][4], vr[W][4], wov[4];
#define STR2(x) #x
#define STR(x) STR2(x)
#define PACE(n) do { asm volatile("s_waitcnt vmcnt(" STR(n) ")" ::: "memory"); __builtin_amdgcn_sched_barrier(0); } while (0)
#define ISSUE(slot, t, n) do { \
      _Pragma("unroll") for (int i_ = 0; i_ < 4; ++i_) { PACE(n); kr[slot][i_] = ldnt(kp + (size_t)(((t) + rot) & (NT - 1)) * (NSW * 16 * DH) + i_ * 4 * DH); __builtin_amdgcn_sched_barrier(0); } \
      _Pragma("unroll") for (int i_ = 0; i_ < 4; ++i_) { PACE(n); vr[slot][i_] = ldnt(vp + (size_t)(((t) + rot) & (NT - 1)) * (NSW * 16 * DH) + i_ * 4 * DH); __builtin_amdgcn_sched_barrier(0); } \
    } while (0)
    __builtin_amdgcn_s_barrier();
    __builtin_amdgcn_sched_barrier(0);
#pragma unroll
    for (int t = 0; t < W; ++t) ISSUE(t, t, INFL_PRO);
    while (LDS_LD(L_QRDY) == 0u) __builtin_amdgcn_s_sleep(1);
    asm volatile("" ::: "memory");
    const s16x8 qf = *reinterpret_cast<const s16x8*>(lds + L_QF + ((n >> 2) * 16 + 4 * g + (n & 3)) * 16);
    const unsigned jm = n & 3;
    const unsigned mask0 = jm == 0 ? 0x0000ffffu : (jm == 1 ? 0xffff0000u : 0u);
    const unsigned mask1 = jm == 2 ? 0x0000ffffu : (jm == 3 ? 0xffff0000u : 0u);
    const bool sel1 = (lane & 1) != 0, sel2 = (lane & 2) != 0;
    float m = -INFINITY, mc = 0.f, l = 0.f;
    f32x4 o0 = {0.f, 0.f, 0.f, 0.f}, o1 = {0.f, 0.f, 0.f, 0.f};

#define TILE(slot) do { \
      float s_[4]; \
      _Pragma("unroll") for (int i_ = 0; i_ < 4; ++i_) { \
        const f32x4 d_ = __builtin_amdgcn_mfma_f32_16x16x32_bf16(__builtin_bit_cast(s16x8, kr[slot][i_]), qf, f32x4{0.f, 0.f, 0.f, 0.f}, 0, 0, 0); \
        const float t0_ = sel1 ? d_[1] : d_[0], t1_ = sel1 ? d_[3] : d_[2]; \
        float u_ = sel2 ? t1_ : t0_; u_ += dpp<XOR1>(u_); u_ += dpp<XOR2>(u_); s_[i_] = u_; } \
      const float mx_ = fmaxf(fmaxf(s_[0], s_[1]), fmaxf(s_[2], s_[3])); \
      if (__builtin_amdgcn_ballot_w64(mx_ > m) != 0) { \
        const float mn_ = fmaxf(m, max_x16_x32(mx_)); \
        const float mcn_ = mn_ * C2; \
        const float al_ = (m == -INFINITY) ? 0.f : __builtin_amdgcn_exp2f(mc - mcn_); \
        l *= al_; m = mn_; mc = mcn_; \
        const float a0_ = asf(__builtin_amdgcn_readlane((int)asu(al_), 0)); \
        const float a1_ = asf(__builtin_amdgcn_readlane((int)asu(al_), 4)); \
        const float a2_ = asf(__builtin_amdgcn_readlane((int)asu(al_), 8)); \
        const float a3_ = asf(__builtin_amdgcn_readlane((int)asu(al_), 12)); \
        const float ao_ = g == 0 ? a0_ : (g == 1 ? a1_ : (g == 2 ? a2_ : a3_)); \
        o0 *= ao_; o1 *= ao_; } \
      _Pragma("unroll") for (int i_ = 0; i_ < 4; ++i_) { \
        const float p_ = __builtin_amdgcn_exp2f(fmaf(s_[i_], C2, -mc)); \
        l += p_; \
        const unsigned pb_ = cvtpk(p_, p_); \
        const u32x2 a_ = {pb_ & mask0, pb_ & mask1}; \
        const u32x2 va_ = {vr[slot][i_][0], vr[slot][i_][1]}, vb_ = {vr[slot][i_][2], vr[slot][i_][3]}; \
        o0 = __builtin_amdgcn_mfma_f32_16x16x16bf16_1k(__builtin_bit_cast(s16x4, a_), __builtin_bit_cast(s16x4, va_), o0, 0, 0, 0); \
        o1 = __builtin_amdgcn_mfma_f32_16x16x16bf16_1k(__builtin_bit_cast(s16x4, a_), __builtin_bit_cast(s16x4, vb_), o1, 0, 0, 0); } \
      asm volatile("" : "+v"(o0), "+v"(o1)); \
    } while (0)

#pragma unroll 1
    for (int t0 = 0; t0 < NT - W; t0 += W) {
      if (t0 == NT - 2 * W && lane == 0) LDS_ST(L_PROG, 1u);
#pragma unroll
      for (int s = 0; s < W; ++s) { TILE(s); __builtin_amdgcn_sched_barrier(0); ISSUE(s, t0 + s + W, INFL_MAIN); }
    }
    TILE(0);
    __builtin_amdgcn_sched_barrier(0);
    {
      const unsigned short* wp = p.wo + (size_t)(kvh * 512 + 32 * sp + (lane >> 3)) * DM + 64 * wave + 8 * (lane & 7);
#pragma unroll
      for (int i = 0; i < 4; ++i) wov[i] = ldw(wp + (size_t)i * 8 * DM);
      __builtin_amdgcn_sched_barrier(0);
    }
#pragma unroll
    for (int s = 1; s < W; ++s) { TILE(s); }
    l = red16(l, l); l = red32(l, l);
    *reinterpret_cast<f32x4*>(lds + L_O + wave * 2048 + g * 512 + n * 32) = o0;
    *reinterpret_cast<f32x4*>(lds + L_O + wave * 2048 + g * 512 + n * 32 + 16) = o1;
    if ((lane & 0x33) == 0) {
      *reinterpret_cast<float*>(lds + L_M + wave * 16 + (n >> 2) * 4) = mc;
      *reinterpret_cast<float*>(lds + L_L + wave * 16 + (n >> 2) * 4) = l;
    }
    STAMP(5);
    BARRIER();
    const unsigned tlo = LDS_LD(L_TAG), thi = LDS_LD(L_TAG + 4);
    {
      const int h = tid >> 7, d = tid & 127;
      float mw[NSW], M = -INFINITY;
#pragma unroll
      for (int w = 0; w < NSW; ++w) { mw[w] = *reinterpret_cast<const float*>(lds + L_M + w * 16 + h * 4); M = fmaxf(M, mw[w]); }
      M = asf(asu(M) & 0xffffff00u);
      float O = 0.f, L = 0.f;
#pragma unroll
      for (int w = 0; w < NSW; ++w) {
        const float wt = __builtin_amdgcn_exp2f(mw[w] - M);
        O = fmaf(wt, *reinterpret_cast<const float*>(lds + L_O + w * 2048 + h * 512 + d * 4), O);
        L = fmaf(wt, *reinterpret_cast<const float*>(lds + L_L + w * 16 + h * 4), L);
      }
      const int j = h * 4 + (d >> 5);
      const unsigned dst = WS_H1 + (unsigned)(kvh + 8 * (b * 16 + j)) * 8192u;
      st_g(ws, dst + sp * 256 + (d & 31) * 8, gran24(O, tlo, thi));
      if ((d & 31) == 0) ST16((u32x4{f24(M, tlo), thi, f24(L, tlo), thi}), dst + 4096 + sp * 16);
    }
    BARRIER();
    {
      float acc[8];
#pragma unroll
      for (int c = 0; c < 8; ++c) acc[c] = 0.f;
#pragma unroll
      for (int i = 0; i < 4; ++i) {
        const float ov = *reinterpret_cast<const float*>(lds + L_OS + (8 * i + (lane >> 3)) * 4);
#pragma unroll
        for (int c = 0; c < 4; ++c) { acc[2 * c] = fmaf(ov, bf_lo(wov[i][c]), acc[2 * c]); acc[2 * c + 1] = fmaf(ov, bf_hi(wov[i][c]), acc[2 * c + 1]); }
      }
#pragma unroll
      for (int c = 0; c < 4; ++c) acc[c] = red32(acc[c], acc[c + 4]);
#pragma unroll
      for (int c = 0; c < 2; ++c) acc[c] = red16(acc[c], acc[c + 2]);
      const float e0 = acc[0] + dpp<ROR8>(acc[0]), e1 = acc[1] + dpp<ROR8>(acc[1]);
      const float sv = (lane & 8) ? e1 : e0;
      const int col = 64 * wave + 8 * (lane & 7) + 4 * (lane >> 5) + 2 * ((lane >> 4) & 1) + ((lane >> 3) & 1);
      st_g(ws, WS_H2 + (unsigned)(b * 128 + (col >> 2)) * 4096u + (kvh * 16 + sp) * 32 + (col & 3) * 8, gran24(sv, tlo, thi));
    }
#undef TILE
#undef ISSUE
#undef PACE
  } else {
    __builtin_amdgcn_s_setprio(3);
    const int qw = wave - NSW;
    STAMP(0);
    const int row0 = qw * 32 * QR + (lane >> 1);
    u32x4 wqv[QR];
    {
      const unsigned short* wp = p.wq + (size_t)row0 * 4096 + kvh * 512 + r * 16 + 8 * (lane & 1);
#pragma unroll
      for (int i = 0; i < QR; ++i) wqv[i] = ldw(wp + (size_t)i * 32 * 4096);
    }
    const u32x4 xa = *reinterpret_cast<const u32x4*>(p.x + 8 * lane);
    const u32x4 xb = *reinterpret_cast<const u32x4*>(p.x + DM + 8 * lane);
    unsigned short xs[QR];
#pragma unroll
    for (int i = 0; i < QR; ++i) xs[i] = p.x[(lane & 1) * DM + row0 + 32 * i];
    u64 ep = 0; unsigned xo = 0;
    if (qw == 0) {
      ep = ld_g(ws, WS_E) + 1ull;
      xo = *reinterpret_cast<const unsigned*>(p.x + b * DM + 4 * cg + 2 * (lane & 1));
      if (lane == 0) { LDS_ST(L_QCNT, 0u); LDS_ST(L_QRDY, 0u); LDS_ST(L_PROG, 0u); }
    }
    __builtin_amdgcn_sched_barrier(0);
    BARRIER();
    {
      float m0, s0, m1, s1;
      ln_stats(xa, m0, s0); ln_stats(xb, m1, s1);
      const float mb = (lane & 1) ? m1 : m0, sd = (lane & 1) ? s1 : s0;
      float qa[16];
#pragma unroll
      for (int c = 0; c < 16; ++c) qa[c] = 0.f;
#pragma unroll
      for (int i = 0; i < QR; ++i) {
        const float a = ln_apply(bf_lo(xs[i]), mb, sd);
        reinterpret_cast<float*>(lds + L_XN)[(lane & 1) * DM + row0 + 32 * i] = a;
        const float ao = dpp<XOR1>(a);
        const float a0 = (lane & 1) ? ao : a, a1 = (lane & 1) ? a : ao;
#pragma unroll
        for (int c = 0; c < 4; ++c) {
          const float lo = bf_lo(wqv[i][c]), hi = bf_hi(wqv[i][c]);
          qa[2 * c] = fmaf(a0, lo, qa[2 * c]); qa[2 * c + 1] = fmaf(a0, hi, qa[2 * c + 1]);
          qa[8 + 2 * c] = fmaf(a1, lo, qa[8 + 2 * c]); qa[8 + 2 * c + 1] = fmaf(a1, hi, qa[8 + 2 * c + 1]);
        }
      }
#pragma unroll
      for (int i = 0; i < 8; ++i) qa[i] = red32(qa[i], qa[i + 8]);
#pragma unroll
      for (int i = 0; i < 4; ++i) qa[i] = red16(qa[i], qa[i + 4]);
#pragma unroll
      for (int i = 0; i < 4; ++i) qa[i] = row_sum8_odd_even(qa[i]);
      if ((lane & 14) == 0)
        *reinterpret_cast<f32x4*>(lds + L_Q + qw * 128 + (lane >> 5) * 64 + (8 * (lane & 1) + 4 * ((lane >> 4) & 1)) * 4) = f32x4{qa[0], qa[1], qa[2], qa[3]};
    }
    asm volatile("s_waitcnt lgkmcnt(0)" ::: "memory");
    if (qw != 0) {
      if (lane == 0) __hip_atomic_fetch_add((lds_u32*)(lds + L_QCNT), 1u, __ATOMIC_RELAXED, __HIP_MEMORY_SCOPE_WORKGROUP);
      return;
    }
    while (LDS_LD(L_QCNT) != (unsigned)(NQW - 1)) { }
    asm volatile("" ::: "memory");
    STAMP(1);
    const unsigned qhi = (unsigned)(ep >> 16), qlo = ((unsigned)ep) << 16;
    const unsigned thi = (unsigned)(ep >> 8), tlo = ((unsigned)ep) << 24;
    {
      float qs = 0.f;
#pragma unroll
      for (int w = 0; w < NQW; ++w) qs += *reinterpret_cast<const float*>(lds + L_Q + w * 128 + (lane & 31) * 4);
      if (lane < 32)
        st_g(ws, WS_Q + (((lane >> 4) * 8 + kvh) * 512 + r * 16 + (lane & 15)) * 8, (ep << 16) | (cvtpk(qs, qs) & 0xffffu));
    }
    {
      const unsigned qb = WS_Q + (b * 8 + kvh) * 4096 + lane * 64;
      const u64 t0 = __builtin_amdgcn_s_memrealtime();
      u32x4 qv[4];
      for (;;) {
        asm volatile("" ::: "memory");
#pragma unroll
        for (int i = 0; i < 4; ++i) qv[i] = LD16(qb + i * 16);
        unsigned bad = 0;
#pragma unroll
        for (int i = 0; i < 4; ++i) bad |= BAD16(qv[i]);
        if (__builtin_amdgcn_ballot_w64(bad != 0) == 0 || timed_out(t0)) break;
      }
      u32x4 f;
#pragma unroll
      for (int i = 0; i < 4; ++i) f[i] = (qv[i][0] & 0xffffu) | (qv[i][2] << 16);
      *reinterpret_cast<u32x4*>(lds + L_QF + lane * 16) = f;
      if (lane == 0) { LDS_ST(L_TAG, tlo); LDS_ST(L_TAG + 4, thi); }
      asm volatile("s_waitcnt lgkmcnt(0)" ::: "memory");
      if (lane == 0) LDS_ST(L_QRDY, 1u);
    }
    STAMP(2);
    u32x4 w2v[7];
    {
      u32x4 w1v[8];
      const int f0 = 224 * kvh + 8 * (r < 28 ? r : 0);
#pragma unroll
      for (int i = 0; i < 8; ++i) w1v[i] = ldf(p.w1 + (size_t)(lane + 64 * i) * DFF + f0);
      const unsigned short* wp = p.w2 + (size_t)(224 * kvh + (lane >> 1)) * DM + 16 * r + 8 * (lane & 1);
#pragma unroll
      for (int i = 0; i < 7; ++i) w2v[i] = ldf(wp + (size_t)i * 32 * DM);
      float ha[16];
#pragma unroll
      for (int c = 0; c < 16; ++c) ha[c] = 0.f;
#pragma unroll
      for (int i = 0; i < 8; ++i) {
        const float a0 = *reinterpret_cast<const float*>(lds + L_XN + (lane + 64 * i) * 4);
        const float a1 = *reinterpret_cast<const float*>(lds + L_XN + 2048 + (lane + 64 * i) * 4);
#pragma unroll
        for (int c = 0; c < 4; ++c) {
          const float lo = bf_lo(w1v[i][c]), hi = bf_hi(w1v[i][c]);
          ha[2 * c] = fmaf(a0, lo, ha[2 * c]); ha[2 * c + 1] = fmaf(a0, hi, ha[2 * c + 1]);
          ha[8 + 2 * c] = fmaf(a1, lo, ha[8 + 2 * c]); ha[8 + 2 * c + 1] = fmaf(a1, hi, ha[8 + 2 * c + 1]);
        }
      }
#pragma unroll
      for (int i = 0; i < 8; ++i) ha[i] = red32(ha[i], ha[i + 8]);
#pragma unroll
      for (int i = 0; i < 4; ++i) ha[i] = red16(ha[i], ha[i + 4]);
#pragma unroll
      for (int i = 0; i < 4; ++i) ha[i] = row_sum16(ha[i]);
      const float hv = (lane & 2) ? ((lane & 1) ? ha[3] : ha[2]) : ((lane & 1) ? ha[1] : ha[0]);
      const float hr = fmaxf(rbf(hv), 0.f);
      if (r < 28 && (lane & 12) == 0)
        st_g(ws, WS_HX + ((kvh * 32 + r) * 16 + (lane >> 5) * 8 + ((lane >> 4) & 1) * 4 + (lane & 3)) * 8, (ep << 16) | (asu(hr) >> 16));
    }
    STAMP(3);
    {
      const unsigned hb = WS_HX + kvh * 4096 + lane * 16;
      const u64 t0 = __builtin_amdgcn_s_memrealtime();
      u32x4 hv[4];
      for (;;) {
        __builtin_amdgcn_s_sleep(64);
        asm volatile("" ::: "memory");
#pragma unroll
        for (int i = 0; i < 4; ++i) hv[i] = LD16(hb + i * 1024);
        const unsigned bad = BAD16(hv[0]) | BAD16(hv[1]) | BAD16(hv[2]) | (lane < 32 ? BAD16(hv[3]) : 0u);
        if (__builtin_amdgcn_ballot_w64(bad != 0) == 0 || timed_out(t0)) break;
      }
#pragma unroll
      for (int i = 0; i < 4; ++i) {
        const int e = 64 * i + lane;
        *reinterpret_cast<f32x2*>(lds + L_HS + ((e >> 2) & 1) * 1024 + ((e >> 3) * 8 + 2 * (e & 3)) * 4) = f32x2{bf_lo(hv[i][0]), bf_lo(hv[i][2])};
      }
      asm volatile("s_waitcnt lgkmcnt(0)" ::: "memory");
      float ya[16];
#pragma unroll
      for (int c = 0; c < 16; ++c) ya[c] = 0.f;
#pragma unroll
      for (int i = 0; i < 7; ++i) {
        const float a0 = *reinterpret_cast<const float*>(lds + L_HS + (32 * i + (lane >> 1)) * 4);
        const float a1 = *reinterpret_cast<const float*>(lds + L_HS + 1024 + (32 * i + (lane >> 1)) * 4);
#pragma unroll
        for (int c = 0; c < 4; ++c) {
          const float lo = bf_lo(w2v[i][c]), hi = bf_hi(w2v[i][c]);
          ya[2 * c] = fmaf(a0, lo, ya[2 * c]); ya[2 * c + 1] = fmaf(a0, hi, ya[2 * c + 1]);
          ya[8 + 2 * c] = fmaf(a1, lo, ya[8 + 2 * c]); ya[8 + 2 * c + 1] = fmaf(a1, hi, ya[8 + 2 * c + 1]);
        }
      }
#pragma unroll
      for (int i = 0; i < 8; ++i) ya[i] = red32(ya[i], ya[i + 8]);
#pragma unroll
      for (int i = 0; i < 4; ++i) ya[i] = red16(ya[i], ya[i + 4]);
#pragma unroll
      for (int i = 0; i < 4; ++i) ya[i] = row_sum8_odd_even(ya[i]);
      const float yv = (lane & 4) ? ((lane & 2) ? ya[3] : ya[2]) : ((lane & 2) ? ya[1] : ya[0]);
      if ((lane & 8) == 0)
        st_g(ws, WS_YX + ((kvh * 32 + r) * 32 + (lane >> 5) * 16 + (lane & 1) * 8 + ((lane >> 4) & 1) * 4 + ((lane >> 1) & 3)) * 8, gran24(yv, tlo, thi));
    }
    STAMP(9);
    while (LDS_LD(L_PROG) == 0u) __builtin_amdgcn_s_sleep(32);
    STAMP(10);
    float gate0, gate1;
    {
      const unsigned yb = WS_YX + (((lane >> 1) & 7) * 32 + (cg >> 2)) * 256 + (b * 16 + (cg & 3) * 4 + 2 * (lane & 1)) * 8;
      const u64 t0 = __builtin_amdgcn_s_memrealtime();
      u32x4 yv;
      for (;;) {
        asm volatile("" ::: "memory");
        yv = LD16(yb);
        if (__builtin_amdgcn_ballot_w64(BAD24(yv) != 0) == 0 || timed_out(t0)) break;
        __builtin_amdgcn_s_sleep(32);
      }
      const float ya = rbf(row_sum8_odd_even(uf24(yv[0]))), yc = rbf(row_sum8_odd_even(uf24(yv[2])));
      gate0 = rbf(ya / (1.f + __expf(-ya))); gate1 = rbf(yc / (1.f + __expf(-yc)));
    }
    asm volatile("" :: "v"(gate0), "v"(gate1));
    STAMP(5);
    BARRIER();
    STAMP(6);
    {
      const unsigned ib = WS_H1 + bid * 8192;
      const u64 t0 = __builtin_amdgcn_s_memrealtime();
      u32x4 ov[4], mv[4];
      for (;;) {
        asm volatile("" ::: "memory");
#pragma unroll
        for (int i = 0; i < 4; ++i) { ov[i] = LD16(ib + (4 * i + g) * 256 + n * 16); mv[i] = LD16(ib + 4096 + (4 * i + g) * 16); }
        unsigned bad = 0;
#pragma unroll
        for (int i = 0; i < 4; ++i) bad |= BAD24(ov[i]) | BAD24(mv[i]);
        if (__builtin_amdgcn_ballot_w64(bad != 0) == 0 || timed_out(t0)) break;
      }
      float M = -INFINITY;
#pragma unroll
      for (int i = 0; i < 4; ++i) M = fmaxf(M, uf24(mv[i][0]));
      M = max_x16_x32(M);
      float L = 0.f, oa = 0.f, ob = 0.f;
#pragma unroll
      for (int i = 0; i < 4; ++i) {
        const float wt = __builtin_amdgcn_exp2f(uf24(mv[i][0]) - M);
        L = fmaf(wt, uf24(mv[i][2]), L);
        oa = fmaf(wt, uf24(ov[i][0]), oa);
        ob = fmaf(wt, uf24(ov[i][2]), ob);
      }
      L = red16(L, L); L = red32(L, L);
      oa = red16(oa, ob); oa = red32(oa, oa);
      if (lane < 32) *reinterpret_cast<float*>(lds + L_OS + (2 * n + g) * 4) = rbf(oa / L);
    }
    STAMP(7);
    BARRIER();
    {
      const unsigned ib = WS_H2 + (b * 128 + cg) * 4096 + lane * 16;
      const u64 t0 = __builtin_amdgcn_s_memrealtime();
      u32x4 sv[4];
      for (;;) {
        asm volatile("" ::: "memory");
#pragma unroll
        for (int i = 0; i < 4; ++i) sv[i] = LD16(ib + i * 1024);
        unsigned bad = 0;
#pragma unroll
        for (int i = 0; i < 4; ++i) bad |= BAD24(sv[i]);
        if (__builtin_amdgcn_ballot_w64(bad != 0) == 0 || timed_out(t0)) break;
      }
      float s0 = (uf24(sv[0][0]) + uf24(sv[1][0])) + (uf24(sv[2][0]) + uf24(sv[3][0]));
      float s1 = (uf24(sv[0][2]) + uf24(sv[1][2])) + (uf24(sv[2][2]) + uf24(sv[3][2]));
      s0 = row_sum8_odd_even(s0); s0 = red16(s0, s0); s0 = red32(s0, s0);
      s1 = row_sum8_odd_even(s1); s1 = red16(s1, s1); s1 = red32(s1, s1);
      const float r0 = bf_lo(xo) + rbf(rbf(s0) * gate0), r1 = bf_hi(xo) + rbf(rbf(s1) * gate1);
      if (lane < 2) *reinterpret_cast<unsigned*>(p.out + b * DM + 4 * cg + 2 * lane) = cvtpk(r0, r1);
      if (bid == 0 && lane == 0) st_g(ws, WS_E, ep);
      STAMP(8);
    }
  }
}

}

extern "C" void kernel_launch(void* const* d_in, const int* in_sizes, int n_in, void* d_out, int out_size, void* d_ws,
                              size_t ws_size, hipStream_t stream) {
  Params p;
  p.x = (const unsigned short*)d_in[0]; p.k = (const unsigned short*)d_in[1]; p.v = (const unsigned short*)d_in[2];
  p.wq = (const unsigned short*)d_in[3]; p.wo = (const unsigned short*)d_in[4];
  p.w1 = (const unsigned short*)d_in[6]; p.w2 = (const unsigned short*)d_in[7];
  p.out = (unsigned short*)d_out; p.ws = (unsigned char*)d_ws + WS_BASE;
  hipLaunchKernelGGL(fused_decode, dim3(256), dim3(NTHR), 0, stream, p);
}
```

```cpp
#include <hip/hip_runtime.h>
#include <hip/hip_bf16.h>
#include <cstdint>
#define W_PLAIN 1

namespace {

typedef unsigned u32x4 __attribute__((ext_vector_type(4)));
typedef unsigned u32x2 __attribute__((ext_vector_type(2)));
typedef float f32x4 __attribute__((ext_vector_type(4)));
typedef float f32x2 __attribute__((ext_vector_type(2)));
typedef __bf16 bf16x2 __attribute__((ext_vector_type(2)));
typedef short s16x8 __attribute__((ext_vector_type(8)));
typedef short s16x4 __attribute__((ext_vector_type(4)));
typedef unsigned long long u64;
#define GAS __attribute__((address_space(1)))
typedef GAS u64 gu64;

constexpr int SK = 32768, DH = 128, DM = 512, DFF = 1792, HKV = 8;
constexpr int NSPLIT = 16, KEYS = SK / NSPLIT;
constexpr int NSW = 8;
#define NQW 4
#define INFL_PRO 3
#define INFL_MAIN 4
constexpr int NTHR = (NSW + NQW) * 64;
constexpr int NT = KEYS / 16 / NSW;
constexpr int W = 4;
constexpr int QR = DM / NQW / 32;
static_assert(NT % W == 0, "ring");

#define WS_BASE 0
constexpr unsigned WS_E = 0;
constexpr unsigned WS_Q = 0x2000;
constexpr unsigned WS_HX = 0x12000;
constexpr unsigned WS_YX = 0x20000;
constexpr unsigned WS_H1 = 0x200000;
constexpr unsigned WS_H2 = 0x400000;
constexpr unsigned WS_END = 0x500000;

constexpr float C2 = 0.08838834764831845f * 1.4426950408889634f;
constexpr u64 SPIN_TICKS = 4000000ull;

constexpr int L_O = 0;
constexpr int L_M = L_O + NSW * 2048;
constexpr int L_L = L_M + NSW * 16;
constexpr int L_Q = L_L + NSW * 16;
constexpr int L_XN = L_Q + 4 * 128;
constexpr int L_HS = L_XN + 4096;
constexpr int L_QF = L_HS + 2048;
constexpr int L_OS = L_QF + 1024;
constexpr int L_TAG = L_OS + 128;
constexpr int L_QCNT = L_TAG + 8;
constexpr int L_QRDY = L_TAG + 12;
constexpr int L_PROG = L_TAG + 16;
constexpr int L_END = L_TAG + 32;

__device__ __forceinline__ float asf(unsigned u) { return __builtin_bit_cast(float, u); }
__device__ __forceinline__ unsigned asu(float f) { return __builtin_bit_cast(unsigned, f); }
__device__ __forceinline__ float bf_lo(unsigned u) { return asf(u << 16); }
__device__ __forceinline__ float bf_hi(unsigned u) { return asf(u & 0xffff0000u); }
__device__ __forceinline__ unsigned cvtpk(float lo, float hi) {
  const f32x2 v = {lo, hi};
  return __builtin_bit_cast(unsigned, __builtin_convertvector(v, bf16x2));
}
__device__ __forceinline__ float rbf(float x) { return bf_lo(cvtpk(x, x)); }
template <int CTRL> __device__ __forceinline__ float dpp(float x) {
  return asf(__builtin_amdgcn_mov_dpp((int)asu(x), CTRL, 0xf, 0xf, true));
}
constexpr int XOR1 = 0xB1, XOR2 = 0x4E, HMIRR = 0x141, MIRR = 0x140, ROR4 = 0x124, ROR8 = 0x128;
__device__ __forceinline__ float red32(float a, float b) {
  auto r = __builtin_amdgcn_permlane32_swap(asu(a), asu(b), false, false);
  return asf((unsigned)r[0]) + asf((unsigned)r[1]);
}
__device__ __forceinline__ float red16(float a, float b) {
  auto r = __builtin_amdgcn_permlane16_swap(asu(a), asu(b), false, false);
  return asf((unsigned)r[0]) + asf((unsigned)r[1]);
}
__device__ __forceinline__ float max_x16_x32(float x) {
  auto s = __builtin_amdgcn_permlane16_swap(asu(x), asu(x), false, false);
  x = fmaxf(asf((unsigned)s[0]), asf((unsigned)s[1]));
  auto t = __builtin_amdgcn_permlane32_swap(asu(x), asu(x), false, false);
  return fmaxf(asf((unsigned)t[0]), asf((unsigned)t[1]));
}
__device__ __forceinline__ float row_sum16(float x) {
  x += dpp<XOR1>(x); x += dpp<XOR2>(x); x += dpp<HMIRR>(x); x += dpp<MIRR>(x); return x;
}
__device__ __forceinline__ float row_sum8_odd_even(float t) {
  t += dpp<ROR8>(t); t += dpp<ROR4>(t); t += dpp<XOR2>(t); return t;
}
__device__ __forceinline__ float wave_sum(float x) { x = row_sum16(x); x = red16(x, x); return red32(x, x); }
__device__ __forceinline__ u32x4 ldnt(const void* p) { return __builtin_nontemporal_load(reinterpret_cast<const u32x4*>(p)); }
__device__ __forceinline__ u32x4 ldpl(const void* p) { return *reinterpret_cast<const u32x4*>(p); }
__device__ __forceinline__ u32x4 ldw(const void* p) {
  return *reinterpret_cast<const u32x4*>(p);
}
__device__ __forceinline__ u32x4 ldf(const void* p) {
  return ldw(p);
}
__device__ __forceinline__ void st_g(unsigned char* ws, unsigned off, u64 v) {
  __hip_atomic_store((gu64*)(ws + off), v, __ATOMIC_RELAXED, __HIP_MEMORY_SCOPE_AGENT);
}
__device__ __forceinline__ u64 ld_g(unsigned char* ws, unsigned off) {
  return __hip_atomic_load((gu64*)(ws + off), __ATOMIC_RELAXED, __HIP_MEMORY_SCOPE_AGENT);
}
__device__ __forceinline__ unsigned f24(float x, unsigned tlo) { const unsigned u = asu(x); return ((u + 0x7fu + ((u >> 8) & 1u)) >> 8) | tlo; }
__device__ __forceinline__ float uf24(unsigned lo) { return asf(lo << 8); }
__device__ __forceinline__ u64 gran24(float x, unsigned tlo, unsigned thi) { return ((u64)thi << 32) | f24(x, tlo); }
#define BAD24(v) (((v)[1] ^ thi) | ((v)[3] ^ thi) | (((v)[0] ^ tlo) >> 24) | (((v)[2] ^ tlo) >> 24))
#define BAD16(v) (((v)[1] ^ qhi) | ((v)[3] ^ qhi) | (((v)[0] ^ qlo) >> 16) | (((v)[2] ^ qlo) >> 16))
#define LD16(off) __builtin_amdgcn_raw_buffer_load_b128(rsrc, (off), 0, 16)
#define ST16(v, off) __builtin_amdgcn_raw_buffer_store_b128((v), rsrc, (off), 0, 16)
__device__ __forceinline__ bool timed_out(u64 t0) { return __builtin_amdgcn_s_memrealtime() - t0 > SPIN_TICKS; }
#define STAMP(i) do {} while (0)
#define BARRIER() do { asm volatile("s_waitcnt lgkmcnt(0)" ::: "memory"); __builtin_amdgcn_s_barrier(); asm volatile("" ::: "memory"); } while (0)
typedef __attribute__((address_space(3))) unsigned lds_u32;
#define LDS_LD(off) __hip_atomic_load((lds_u32*)(lds + (off)), __ATOMIC_RELAXED, __HIP_MEMORY_SCOPE_WORKGROUP)
#define LDS_ST(off, v) __hip_atomic_store((lds_u32*)(lds + (off)), (v), __ATOMIC_RELAXED, __HIP_MEMORY_SCOPE_WORKGROUP)

__device__ __forceinline__ void ln_stats(u32x4 xv, float& mb, float& sd) {
  float e[8];
#pragma unroll
  for (int i = 0; i < 4; ++i) { e[2 * i] = bf_lo(xv[i]); e[2 * i + 1] = bf_hi(xv[i]); }
  float s = ((e[0] + e[1]) + (e[2] + e[3])) + ((e[4] + e[5]) + (e[6] + e[7]));
  const float mean = wave_sum(s) * (1.0f / DM);
  float q = 0.f;
#pragma unroll
  for (int i = 0; i < 8; ++i) { const float d = e[i] - mean; q = fmaf(d, d, q); }
  const float var = wave_sum(q) * (1.0f / DM);
  mb = rbf(mean);
  sd = rbf(sqrtf(rbf(rbf(var) + 1e-5f)));
}
__device__ __forceinline__ float ln_apply(float x, float mb, float sd) { return rbf(rbf(x - mb) / sd); }

struct Params {
  const unsigned short* x; const unsigned short* k; const unsigned short* v; const unsigned short* wq;
  const unsigned short* wo; const unsigned short* w1; const unsigned short* w2; unsigned short* out; unsigned char* ws;
};

__global__ void __launch_bounds__(NTHR, 1) fused_decode(Params p) {
  __shared__ __attribute__((aligned(16))) unsigned char lds[L_END];
  const int tid = threadIdx.x, lane = tid & 63;
  const int wave = __builtin_amdgcn_readfirstlane(tid >> 6);
  const int bid = blockIdx.x, kvh = bid & 7, r = bid >> 3, b = r >> 4, sp = r & 15;
  unsigned char* ws = p.ws;
  const auto rsrc = __builtin_amdgcn_make_buffer_rsrc(ws, 0, WS_END, 0x00020000);
  const int n = lane & 15, g = lane >> 4;
  const int cg = kvh * 16 + sp;

  if (wave < NSW) {
    u64 st0 = 0, st1 = 0, st2 = 0, st3 = 0;
    const size_t kvbase = ((size_t)(b * HKV + kvh) * SK + (size_t)sp * KEYS + (size_t)wave * 16) * DH;
    const unsigned short* kp = p.k + kvbase + (size_t)(n >> 2) * DH + 8 * (4 * g + (n & 3));
    const unsigned short* vp = p.v + kvbase + 8 * lane;
    const int rot = (bid * 5 + (bid >> 3) * 11) & (NT - 1);
    u32x4 kr[W][4], vr[W][4], wov[4];
#define STR2(x) #x
#define STR(x) STR2(x)
#define PACE(n) do { asm volatile("s_waitcnt vmcnt(" STR(n) ")" ::: "memory"); __builtin_amdgcn_sched_barrier(0); } while (0)
#define ISSUE(slot, t, n) do { \
      _Pragma("unroll") for (int i_ = 0; i_ < 4; ++i_) { PACE(n); kr[slot][i_] = ldnt(kp + (size_t)(((t) + rot) & (NT - 1)) * (NSW * 16 * DH) + i_ * 4 * DH); __builtin_amdgcn_sched_barrier(0); } \
      _Pragma("unroll") for (int i_ = 0; i_ < 4; ++i_) { PACE(n); vr[slot][i_] = ldpl(vp + (size_t)(((t) + rot) & (NT - 1)) * (NSW * 16 * DH) + i_ * 4 * DH); __builtin_amdgcn_sched_barrier(0); } \
    } while (0)
    __builtin_amdgcn_s_barrier();
    __builtin_amdgcn_sched_barrier(0);
#pragma unroll
    for (int t = 0; t < W; ++t) ISSUE(t, t, INFL_PRO);
    while (LDS_LD(L_QRDY) == 0u) __builtin_amdgcn_s_sleep(1);
    asm volatile("" ::: "memory");
    const s16x8 qf = *reinterpret_cast<const s16x8*>(lds + L_QF + ((n >> 2) * 16 + 4 * g + (n & 3)) * 16);
    const unsigned jm = n & 3;
    const unsigned mask0 = jm == 0 ? 0x0000ffffu : (jm == 1 ? 0xffff0000u : 0u);
    const unsigned mask1 = jm == 2 ? 0x0000ffffu : (jm == 3 ? 0xffff0000u : 0u);
    const bool sel1 = (lane & 1) != 0, sel2 = (lane & 2) != 0;
    float m = -INFINITY, mc = 0.f, l = 0.f;
    f32x4 o0 = {0.f, 0.f, 0.f, 0.f}, o1 = {0.f, 0.f, 0.f, 0.f};

#define TILE(slot) do { \
      float s_[4]; \
      _Pragma("unroll") for (int i_ = 0; i_ < 4; ++i_) { \
        const f32x4 d_ = __builtin_amdgcn_mfma_f32_16x16x32_bf16(__builtin_bit_cast(s16x8, kr[slot][i_]), qf, f32x4{0.f, 0.f, 0.f, 0.f}, 0, 0, 0); \
        const float t0_ = sel1 ? d_[1] : d_[0], t1_ = sel1 ? d_[3] : d_[2]; \
        float u_ = sel2 ? t1_ : t0_; u_ += dpp<XOR1>(u_); u_ += dpp<XOR2>(u_); s_[i_] = u_; } \
      const float mx_ = fmaxf(fmaxf(s_[0], s_[1]), fmaxf(s_[2], s_[3])); \
      if (__builtin_amdgcn_ballot_w64(mx_ > m) != 0) { \
        const float mn_ = fmaxf(m, max_x16_x32(mx_)); \
        const float mcn_ = mn_ * C2; \
        const float al_ = (m == -INFINITY) ? 0.f : __builtin_amdgcn_exp2f(mc - mcn_); \
        l *= al_; m = mn_; mc = mcn_; \
        const float a0_ = asf(__builtin_amdgcn_readlane((int)asu(al_), 0)); \
        const float a1_ = asf(__builtin_amdgcn_readlane((int)asu(al_), 4)); \
        const float a2_ = asf(__builtin_amdgcn_readlane((int)asu(al_), 8)); \
        const float a3_ = asf(__builtin_amdgcn_readlane((int)asu(al_), 12)); \
        const float ao_ = g == 0 ? a0_ : (g == 1 ? a1_ : (g == 2 ? a2_ : a3_)); \
        o0 *= ao_; o1 *= ao_; } \
      _Pragma("unroll") for (int i_ = 0; i_ < 4; ++i_) { \
        const float p_ = __builtin_amdgcn_exp2f(fmaf(s_[i_], C2, -mc)); \
        l += p_; \
        const unsigned pb_ = cvtpk(p_, p_); \
        const u32x2 a_ = {pb_ & mask0, pb_ & mask1}; \
        const u32x2 va_ = {vr[slot][i_][0], vr[slot][i_][1]}, vb_ = {vr[slot][i_][2], vr[slot][i_][3]}; \
        o0 = __builtin_amdgcn_mfma_f32_16x16x16bf16_1k(__builtin_bit_cast(s16x4, a_), __builtin_bit_cast(s16x4, va_), o0, 0, 0, 0); \
        o1 = __builtin_amdgcn_mfma_f32_16x16x16bf16_1k(__builtin_bit_cast(s16x4, a_), __builtin_bit_cast(s16x4, vb_), o1, 0, 0, 0); } \
      asm volatile("" : "+v"(o0), "+v"(o1)); \
    } while (0)

#pragma unroll 1
    for (int t0 = 0; t0 < NT - W; t0 += W) {
      if (t0 == NT - 2 * W && lane == 0) LDS_ST(L_PROG, 1u);
#pragma unroll
      for (int s = 0; s < W; ++s) { TILE(s); __builtin_amdgcn_sched_barrier(0); ISSUE(s, t0 + s + W, INFL_MAIN); }
    }
    TILE(0);
    __builtin_amdgcn_sched_barrier(0);
    {
      const unsigned short* wp = p.wo + (size_t)(kvh * 512 + 32 * sp + (lane >> 3)) * DM + 64 * wave + 8 * (lane & 7);
#pragma unroll
      for (int i = 0; i < 4; ++i) wov[i] = ldw(wp + (size_t)i * 8 * DM);
      __builtin_amdgcn_sched_barrier(0);
    }
#pragma unroll
    for (int s = 1; s < W; ++s) { TILE(s); }
    l = red16(l, l); l = red32(l, l);
    *reinterpret_cast<f32x4*>(lds + L_O + wave * 2048 + g * 512 + n * 32) = o0;
    *reinterpret_cast<f32x4*>(lds + L_O + wave * 2048 + g * 512 + n * 32 + 16) = o1;
    if ((lane & 0x33) == 0) {
      *reinterpret_cast<float*>(lds + L_M + wave * 16 + (n >> 2) * 4) = mc;
      *reinterpret_cast<float*>(lds + L_L + wave * 16 + (n >> 2) * 4) = l;
    }
    STAMP(5);
    BARRIER();
    const unsigned tlo = LDS_LD(L_TAG), thi = LDS_LD(L_TAG + 4);
    {
      const int h = tid >> 7, d = tid & 127;
      float mw[NSW], M = -INFINITY;
#pragma unroll
      for (int w = 0; w < NSW; ++w) { mw[w] = *reinterpret_cast<const float*>(lds + L_M + w * 16 + h * 4); M = fmaxf(M, mw[w]); }
      M = asf(asu(M) & 0xffffff00u);
      float O = 0.f, L = 0.f;
#pragma unroll
      for (int w = 0; w < NSW; ++w) {
        const float wt = __builtin_amdgcn_exp2f(mw[w] - M);
        O = fmaf(wt, *reinterpret_cast<const float*>(lds + L_O + w * 2048 + h * 512 + d * 4), O);
        L = fmaf(wt, *reinterpret_cast<const float*>(lds + L_L + w * 16 + h * 4), L);
      }
      const int j = h * 4 + (d >> 5);
      const unsigned dst = WS_H1 + (unsigned)(kvh + 8 * (b * 16 + j)) * 8192u;
      st_g(ws, dst + sp * 256 + (d & 31) * 8, gran24(O, tlo, thi));
      if ((d & 31) == 0) ST16((u32x4{f24(M, tlo), thi, f24(L, tlo), thi}), dst + 4096 + sp * 16);
    }
    BARRIER();
    {
      float acc[8];
#pragma unroll
      for (int c = 0; c < 8; ++c) acc[c] = 0.f;
#pragma unroll
      for (int i = 0; i < 4; ++i) {
        const float ov = *reinterpret_cast<const float*>(lds + L_OS + (8 * i + (lane >> 3)) * 4);
#pragma unroll
        for (int c = 0; c < 4; ++c) { acc[2 * c] = fmaf(ov, bf_lo(wov[i][c]), acc[2 * c]); acc[2 * c + 1] = fmaf(ov, bf_hi(wov[i][c]), acc[2 * c + 1]); }
      }
#pragma unroll
      for (int c = 0; c < 4; ++c) acc[c] = red32(acc[c], acc[c + 4]);
#pragma unroll
      for (int c = 0; c < 2; ++c) acc[c] = red16(acc[c], acc[c + 2]);
      const float e0 = acc[0] + dpp<ROR8>(acc[0]), e1 = acc[1] + dpp<ROR8>(acc[1]);
      const float sv = (lane & 8) ? e1 : e0;
      const int col = 64 * wave + 8 * (lane & 7) + 4 * (lane >> 5) + 2 * ((lane >> 4) & 1) + ((lane >> 3) & 1);
      st_g(ws, WS_H2 + (unsigned)(b * 128 + (col >> 2)) * 4096u + (kvh * 16 + sp) * 32 + (col & 3) * 8, gran24(sv, tlo, thi));
    }
#undef TILE
#undef ISSUE
#undef PACE
  } else {
    __builtin_amdgcn_s_setprio(3);
    const int qw = wave - NSW;
    STAMP(0);
    const int row0 = qw * 32 * QR + (lane >> 1);
    u32x4 wqv[QR];
    {
      const unsigned short* wp = p.wq + (size_t)row0 * 4096 + kvh * 512 + r * 16 + 8 * (lane & 1);
#pragma unroll
      for (int i = 0; i < QR; ++i) wqv[i] = ldw(wp + (size_t)i * 32 * 4096);
    }
    const u32x4 xa = *reinterpret_cast<const u32x4*>(p.x + 8 * lane);
    const u32x4 xb = *reinterpret_cast<const u32x4*>(p.x + DM + 8 * lane);
    unsigned short xs[QR];
#pragma unroll
    for (int i = 0; i < QR; ++i) xs[i] = p.x[(lane & 1) * DM + row0 + 32 * i];
    u64 ep = 0; unsigned xo = 0;
    if (qw == 0) {
      ep = ld_g(ws, WS_E) + 1ull;
      xo = *reinterpret_cast<const unsigned*>(p.x + b * DM + 4 * cg + 2 * (lane & 1));
      if (lane == 0) { LDS_ST(L_QCNT, 0u); LDS_ST(L_QRDY, 0u); LDS_ST(L_PROG, 0u); }
    }
    __builtin_amdgcn_sched_barrier(0);
    BARRIER();
    {
      float m0, s0, m1, s1;
      ln_stats(xa, m0, s0); ln_stats(xb, m1, s1);
      const float mb = (lane & 1) ? m1 : m0, sd = (lane & 1) ? s1 : s0;
      float qa[16];
#pragma unroll
      for (int c = 0; c < 16; ++c) qa[c] = 0.f;
#pragma unroll
      for (int i = 0; i < QR; ++i) {
        const float a = ln_apply(bf_lo(xs[i]), mb, sd);
        reinterpret_cast<float*>(lds + L_XN)[(lane & 1) * DM + row0 + 32 * i] = a;
        const float ao = dpp<XOR1>(a);
        const float a0 = (lane & 1) ? ao : a, a1 = (lane & 1) ? a : ao;
#pragma unroll
        for (int c = 0; c < 4; ++c) {
          const float lo = bf_lo(wqv[i][c]), hi = bf_hi(wqv[i][c]);
          qa[2 * c] = fmaf(a0, lo, qa[2 * c]); qa[2 * c + 1] = fmaf(a0, hi, qa[2 * c + 1]);
          qa[8 + 2 * c] = fmaf(a1, lo, qa[8 + 2 * c]); qa[8 + 2 * c + 1] = fmaf(a1, hi, qa[8 + 2 * c + 1]);
        }
      }
#pragma unroll
      for (int i = 0; i < 8; ++i) qa[i] = red32(qa[i], qa[i + 8]);
#pragma unroll
      for (int i = 0; i < 4; ++i) qa[i] = red16(qa[i], qa[i + 4]);
#pragma unroll
      for (int i = 0; i < 4; ++i) qa[i] = row_sum8_odd_even(qa[i]);
      if ((lane & 14) == 0)
        *reinterpret_cast<f32x4*>(lds + L_Q + qw * 128 + (lane >> 5) * 64 + (8 * (lane & 1) + 4 * ((lane >> 4) & 1)) * 4) = f32x4{qa[0], qa[1], qa[2], qa[3]};
    }
    asm volatile("s_waitcnt lgkmcnt(0)" ::: "memory");
    if (qw != 0) {
      if (lane == 0) __hip_atomic_fetch_add((lds_u32*)(lds + L_QCNT), 1u, __ATOMIC_RELAXED, __HIP_MEMORY_SCOPE_WORKGROUP);
      return;
    }
    while (LDS_LD(L_QCNT) != (unsigned)(NQW - 1)) { }
    asm volatile("" ::: "memory");
    STAMP(1);
    const unsigned qhi = (unsigned)(ep >> 16), qlo = ((unsigned)ep) << 16;
    const unsigned thi = (unsigned)(ep >> 8), tlo = ((unsigned)ep) << 24;
    {
      float qs = 0.f;
#pragma unroll
      for (int w = 0; w < NQW; ++w) qs += *reinterpret_cast<const float*>(lds + L_Q + w * 128 + (lane & 31) * 4);
      if (lane < 32)
        st_g(ws, WS_Q + (((lane >> 4) * 8 + kvh) * 512 + r * 16 + (lane & 15)) * 8, (ep << 16) | (cvtpk(qs, qs) & 0xffffu));
    }
    {
      const unsigned qb = WS_Q + (b * 8 + kvh) * 4096 + lane * 64;
      const u64 t0 = __builtin_amdgcn_s_memrealtime();
      u32x4 qv[4];
      for (;;) {
        asm volatile("" ::: "memory");
#pragma unroll
        for (int i = 0; i < 4; ++i) qv[i] = LD16(qb + i * 16);
        unsigned bad = 0;
#pragma unroll
        for (int i = 0; i < 4; ++i) bad |= BAD16(qv[i]);
        if (__builtin_amdgcn_ballot_w64(bad != 0) == 0 || timed_out(t0)) break;
      }
      u32x4 f;
#pragma unroll
      for (int i = 0; i < 4; ++i) f[i] = (qv[i][0] & 0xffffu) | (qv[i][2] << 16);
      *reinterpret_cast<u32x4*>(lds + L_QF + lane * 16) = f;
      if (lane == 0) { LDS_ST(L_TAG, tlo); LDS_ST(L_TAG + 4, thi); }
      asm volatile("s_waitcnt lgkmcnt(0)" ::: "memory");
      if (lane == 0) LDS_ST(L_QRDY, 1u);
    }
    STAMP(2);
    u32x4 w2v[7];
    {
      u32x4 w1v[8];
      const int f0 = 224 * kvh + 8 * (r < 28 ? r : 0);
#pragma unroll
      for (int i = 0; i < 8; ++i) w1v[i] = ldf(p.w1 + (size_t)(lane + 64 * i) * DFF + f0);
      const unsigned short* wp = p.w2 + (size_t)(224 * kvh + (lane >> 1)) * DM + 16 * r + 8 * (lane & 1);
#pragma unroll
      for (int i = 0; i < 7; ++i) w2v[i] = ldf(wp + (size_t)i * 32 * DM);
      float ha[16];
#pragma unroll
      for (int c = 0; c < 16; ++c) ha[c] = 0.f;
#pragma unroll
      for (int i = 0; i < 8; ++i) {
        const float a0 = *reinterpret_cast<const float*>(lds + L_XN + (lane + 64 * i) * 4);
        const float a1 = *reinterpret_cast<const float*>(lds + L_XN + 2048 + (lane + 64 * i) * 4);
#pragma unroll
        for (int c = 0; c < 4; ++c) {
          const float lo = bf_lo(w1v[i][c]), hi = bf_hi(w1v[i][c]);
          ha[2 * c] = fmaf(a0, lo, ha[2 * c]); ha[2 * c + 1] = fmaf(a0, hi, ha[2 * c + 1]);
          ha[8 + 2 * c] = fmaf(a1, lo, ha[8 + 2 * c]); ha[8 + 2 * c + 1] = fmaf(a1, hi, ha[8 + 2 * c + 1]);
        }
      }
#pragma unroll
      for (int i = 0; i < 8; ++i) ha[i] = red32(ha[i], ha[i + 8]);
#pragma unroll
      for (int i = 0; i < 4; ++i) ha[i] = red16(ha[i], ha[i + 4]);
#pragma unroll
      for (int i = 0; i < 4; ++i) ha[i] = row_sum16(ha[i]);
      const float hv = (lane & 2) ? ((lane & 1) ? ha[3] : ha[2]) : ((lane & 1) ? ha[1] : ha[0]);
      const float hr = fmaxf(rbf(hv), 0.f);
      if (r < 28 && (lane & 12) == 0)
        st_g(ws, WS_HX + ((kvh * 32 + r) * 16 + (lane >> 5) * 8 + ((lane >> 4) & 1) * 4 + (lane & 3)) * 8, (ep << 16) | (asu(hr) >> 16));
    }
    STAMP(3);
    {
      const unsigned hb = WS_HX + kvh * 4096 + lane * 16;
      const u64 t0 = __builtin_amdgcn_s_memrealtime();
      u32x4 hv[4];
      for (;;) {
        __builtin_amdgcn_s_sleep(64);
        asm volatile("" ::: "memory");
#pragma unroll
        for (int i = 0; i < 4; ++i) hv[i] = LD16(hb + i * 1024);
        const unsigned bad = BAD16(hv[0]) | BAD16(hv[1]) | BAD16(hv[2]) | (lane < 32 ? BAD16(hv[3]) : 0u);
        if (__builtin_amdgcn_ballot_w64(bad != 0) == 0 || timed_out(t0)) break;
      }
#pragma unroll
      for (int i = 0; i < 4; ++i) {
        const int e = 64 * i + lane;
        *reinterpret_cast<f32x2*>(lds + L_HS + ((e >> 2) & 1) * 1024 + ((e >> 3) * 8 + 2 * (e & 3)) * 4) = f32x2{bf_lo(hv[i][0]), bf_lo(hv[i][2])};
      }
      asm volatile("s_waitcnt lgkmcnt(0)" ::: "memory");
      float ya[16];
#pragma unroll
      for (int c = 0; c < 16; ++c) ya[c] = 0.f;
#pragma unroll
      for (int i = 0; i < 7; ++i) {
        const float a0 = *reinterpret_cast<const float*>(lds + L_HS + (32 * i + (lane >> 1)) * 4);
        const float a1 = *reinterpret_cast<const float*>(lds + L_HS + 1024 + (32 * i + (lane >> 1)) * 4);
#pragma unroll
        for (int c = 0; c < 4; ++c) {
          const float lo = bf_lo(w2v[i][c]), hi = bf_hi(w2v[i][c]);
          ya[2 * c] = fmaf(a0, lo, ya[2 * c]); ya[2 * c + 1] = fmaf(a0, hi, ya[2 * c + 1]);
          ya[8 + 2 * c] = fmaf(a1, lo, ya[8 + 2 * c]); ya[8 + 2 * c + 1] = fmaf(a1, hi, ya[8 + 2 * c + 1]);
        }
      }
#pragma unroll
      for (int i = 0; i < 8; ++i) ya[i] = red32(ya[i], ya[i + 8]);
#pragma unroll
      for (int i = 0; i < 4; ++i) ya[i] = red16(ya[i], ya[i + 4]);
#pragma unroll
      for (int i = 0; i < 4; ++i) ya[i] = row_sum8_odd_even(ya[i]);
      const float yv = (lane & 4) ? ((lane & 2) ? ya[3] : ya[2]) : ((lane & 2) ? ya[1] : ya[0]);
      if ((lane & 8) == 0)
        st_g(ws, WS_YX + ((kvh * 32 + r) * 32 + (lane >> 5) * 16 + (lane & 1) * 8 + ((lane >> 4) & 1) * 4 + ((lane >> 1) & 3)) * 8, gran24(yv, tlo, thi));
    }
    STAMP(9);
    while (LDS_LD(L_PROG) == 0u) __builtin_amdgcn_s_sleep(32);
    STAMP(10);
    float gate0, gate1;
    {
      const unsigned yb = WS_YX + (((lane >> 1) & 7) * 32 + (cg >> 2)) * 256 + (b * 16 + (cg & 3) * 4 + 2 * (lane & 1)) * 8;
      const u64 t0 = __builtin_amdgcn_s_memrealtime();
      u32x4 yv;
      for (;;) {
        asm volatile("" ::: "memory");
        yv = LD16(yb);
        if (__builtin_amdgcn_ballot_w64(BAD24(yv) != 0) == 0 || timed_out(t0)) break;
        __builtin_amdgcn_s_sleep(32);
      }
      const float ya = rbf(row_sum8_odd_even(uf24(yv[0]))), yc = rbf(row_sum8_odd_even(uf24(yv[2])));
      gate0 = rbf(ya / (1.f + __expf(-ya))); gate1 = rbf(yc / (1.f + __expf(-yc)));
    }
    asm volatile("" :: "v"(gate0), "v"(gate1));
    STAMP(5);
    BARRIER();
    STAMP(6);
    {
      const unsigned ib = WS_H1 + bid * 8192;
      const u64 t0 = __builtin_amdgcn_s_memrealtime();
      u32x4 ov[4], mv[4];
      for (;;) {
        asm volatile("" ::: "memory");
#pragma unroll
        for (int i = 0; i < 4; ++i) { ov[i] = LD16(ib + (4 * i + g) * 256 + n * 16); mv[i] = LD16(ib + 4096 + (4 * i + g) * 16); }
        unsigned bad = 0;
#pragma unroll
        for (int i = 0; i < 4; ++i) bad |= BAD24(ov[i]) | BAD24(mv[i]);
        if (__builtin_amdgcn_ballot_w64(bad != 0) == 0 || timed_out(t0)) break;
      }
      float M = -INFINITY;
#pragma unroll
      for (int i = 0; i < 4; ++i) M = fmaxf(M, uf24(mv[i][0]));
      M = max_x16_x32(M);
      float L = 0.f, oa = 0.f, ob = 0.f;
#pragma unroll
      for (int i = 0; i < 4; ++i) {
        const float wt = __builtin_amdgcn_exp2f(uf24(mv[i][0]) - M);
        L = fmaf(wt, uf24(mv[i][2]), L);
        oa = fmaf(wt, uf24(ov[i][0]), oa);
        ob = fmaf(wt, uf24(ov[i][2]), ob);
      }
      L = red16(L, L); L = red32(L, L);
      oa = red16(oa, ob); oa = red32(oa, oa);
      if (lane < 32) *reinterpret_cast<float*>(lds + L_OS + (2 * n + g) * 4) = rbf(oa / L);
    }
    STAMP(7);
    BARRIER();
    {
      const unsigned ib = WS_H2 + (b * 128 + cg) * 4096 + lane * 16;
      const u64 t0 = __builtin_amdgcn_s_memrealtime();
      u32x4 sv[4];
      for (;;) {
        asm volatile("" ::: "memory");
#pragma unroll
        for (int i = 0; i < 4; ++i) sv[i] = LD16(ib + i * 1024);
        unsigned bad = 0;
#pragma unroll
        for (int i = 0; i < 4; ++i) bad |= BAD24(sv[i]);
        if (__builtin_amdgcn_ballot_w64(bad != 0) == 0 || timed_out(t0)) break;
      }
      float s0 = (uf24(sv[0][0]) + uf24(sv[1][0])) + (uf24(sv[2][0]) + uf24(sv[3][0]));
      float s1 = (uf24(sv[0][2]) + uf24(sv[1][2])) + (uf24(sv[2][2]) + uf24(sv[3][2]));
      s0 = row_sum8_odd_even(s0); s0 = red16(s0, s0); s0 = red32(s0, s0);
      s1 = row_sum8_odd_even(s1); s1 = red16(s1, s1); s1 = red32(s1, s1);
      const float r0 = bf_lo(xo) + rbf(rbf(s0) * gate0), r1 = bf_hi(xo) + rbf(rbf(s1) * gate1);
      if (lane < 2) *reinterpret_cast<unsigned*>(p.out + b * DM + 4 * cg + 2 * lane) = cvtpk(r0, r1);
      if (bid == 0 && lane == 0) st_g(ws, WS_E, ep);
      STAMP(8);
    }
  }
}

}

extern "C" void kernel_launch(void* const* d_in, const int* in_sizes, int n_in, void* d_out, int out_size, void* d_ws,
                              size_t ws_size, hipStream_t stream) {
  Params p;
  p.x = (const unsigned short*)d_in[0]; p.k = (const unsigned short*)d_in[1]; p.v = (const unsigned short*)d_in[2];
  p.wq = (const unsigned short*)d_in[3]; p.wo = (const unsigned short*)d_in[4];
  p.w1 = (const unsigned short*)d_in[6]; p.w2 = (const unsigned short*)d_in[7];
  p.out = (unsigned short*)d_out; p.ws = (unsigned char*)d_ws + WS_BASE;
  hipLaunchKernelGGL(fused_decode, dim3(256), dim3(NTHR), 0, stream, p);
}
```

```cpp
#include <hip/hip_runtime.h>
#include <hip/hip_bf16.h>
#include <cstdint>

namespace {

typedef unsigned u32x4 __attribute__((ext_vector_type(4)));
typedef unsigned u32x2 __attribute__((ext_vector_type(2)));
typedef float f32x4 __attribute__((ext_vector_type(4)));
typedef float f32x2 __attribute__((ext_vector_type(2)));
typedef __bf16 bf16x2 __attribute__((ext_vector_type(2)));
typedef short s16x8 __attribute__((ext_vector_type(8)));
typedef short s16x4 __attribute__((ext_vector_type(4)));
typedef unsigned long long u64;
#define GAS __attribute__((address_space(1)))
typedef GAS u64 gu64;

constexpr int SK = 32768, DH = 128, DM = 512, DFF = 1792, HKV = 8;
constexpr int NSPLIT = 16, KEYS = SK / NSPLIT;
constexpr int NSW = 8;
constexpr int NQW = 4;
#define INFL_PRO 3
#define INFL_MAIN 4
constexpr int NTHR = (NSW + NQW) * 64;
constexpr int NTILE = KEYS / 16;
constexpr int W = 4;
constexpr int QR = DM / NQW / 32;

constexpr unsigned WS_E = 0;
constexpr unsigned WS_Q = 0x2000;
constexpr unsigned WS_HX = 0x12000;
constexpr unsigned WS_YX = 0x20000;
constexpr unsigned WS_H1 = 0x200000;
constexpr unsigned WS_H2 = 0x400000;
constexpr unsigned WS_END = 0x440000;

constexpr float C2 = 0.08838834764831845f * 1.4426950408889634f;
constexpr u64 SPIN_TICKS = 4000000ull;

constexpr int L_O = 0;
constexpr int L_M = L_O + NSW * 2048;
constexpr int L_L = L_M + NSW * 16;
constexpr int L_Q = L_L + NSW * 16;
constexpr int L_HP = L_Q + 4 * 128;
constexpr int L_HS = L_HP + 4 * 64;
constexpr int L_QF = L_HS + 2048;
constexpr int L_OP = L_QF + 1024;
constexpr int L_ML2 = L_OP + NSW * 512;
constexpr int L_WO = L_ML2 + NSW * 8;
constexpr int L_TAG = L_WO + 128 * 256;
constexpr int L_GATE = L_TAG + 16;
constexpr int L_QCNT = L_GATE + 16;
constexpr int L_QRDY = L_QCNT + 4;
constexpr int L_PROG = L_QCNT + 8;
constexpr int L_TICK = L_QCNT + 12;
constexpr int L_TRDY = L_QCNT + 16;
constexpr int L_END = L_QCNT + 32;

__device__ __forceinline__ float asf(unsigned u) { return __builtin_bit_cast(float, u); }
__device__ __forceinline__ unsigned asu(float f) { return __builtin_bit_cast(unsigned, f); }
__device__ __forceinline__ float bf_lo(unsigned u) { return asf(u << 16); }
__device__ __forceinline__ float bf_hi(unsigned u) { return asf(u & 0xffff0000u); }
__device__ __forceinline__ unsigned cvtpk(float lo, float hi) {
  const f32x2 v = {lo, hi};
  return __builtin_bit_cast(unsigned, __builtin_convertvector(v, bf16x2));
}
__device__ __forceinline__ float rbf(float x) { return bf_lo(cvtpk(x, x)); }
template <int CTRL> __device__ __forceinline__ float dpp(float x) {
  return asf(__builtin_amdgcn_mov_dpp((int)asu(x), CTRL, 0xf, 0xf, true));
}
constexpr int XOR1 = 0xB1, XOR2 = 0x4E, HMIRR = 0x141, MIRR = 0x140, ROR4 = 0x124, ROR8 = 0x128;
__device__ __forceinline__ float red32(float a, float b) {
  auto r = __builtin_amdgcn_permlane32_swap(asu(a), asu(b), false, false);
  return asf((unsigned)r[0]) + asf((unsigned)r[1]);
}
__device__ __forceinline__ float red16(float a, float b) {
  auto r = __builtin_amdgcn_permlane16_swap(asu(a), asu(b), false, false);
  return asf((unsigned)r[0]) + asf((unsigned)r[1]);
}
__device__ __forceinline__ float max_x16_x32(float x) {
  auto s = __builtin_amdgcn_permlane16_swap(asu(x), asu(x), false, false);
  x = fmaxf(asf((unsigned)s[0]), asf((unsigned)s[1]));
  auto t = __builtin_amdgcn_permlane32_swap(asu(x), asu(x), false, false);
  return fmaxf(asf((unsigned)t[0]), asf((unsigned)t[1]));
}
__device__ __forceinline__ float row_sum16(float x) {
  x += dpp<XOR1>(x); x += dpp<XOR2>(x); x += dpp<HMIRR>(x); x += dpp<MIRR>(x); return x;
}
__device__ __forceinline__ float row_sum8_odd_even(float t) {
  t += dpp<ROR8>(t); t += dpp<ROR4>(t); t += dpp<XOR2>(t); return t;
}
__device__ __forceinline__ float wave_sum(float x) { x = row_sum16(x); x = red16(x, x); return red32(x, x); }
__device__ __forceinline__ u32x4 ldnt(const void* p) { return __builtin_nontemporal_load(reinterpret_cast<const u32x4*>(p)); }
__device__ __forceinline__ void st_g(unsigned char* ws, unsigned off, u64 v) {
  __hip_atomic_store((gu64*)(ws + off), v, __ATOMIC_RELAXED, __HIP_MEMORY_SCOPE_AGENT);
}
__device__ __forceinline__ u64 ld_g(unsigned char* ws, unsigned off) {
  return __hip_atomic_load((gu64*)(ws + off), __ATOMIC_RELAXED, __HIP_MEMORY_SCOPE_AGENT);
}
__device__ __forceinline__ unsigned f24(float x, unsigned tlo) { const unsigned u = asu(x); return ((u + 0x7fu + ((u >> 8) & 1u)) >> 8) | tlo; }
__device__ __forceinline__ float uf24(unsigned lo) { return asf(lo << 8); }
__device__ __forceinline__ u64 gran24(float x, unsigned tlo, unsigned thi) { return ((u64)thi << 32) | f24(x, tlo); }
#define BAD24(v) (((v)[1] ^ thi) | ((v)[3] ^ thi) | (((v)[0] ^ tlo) >> 24) | (((v)[2] ^ tlo) >> 24))
#define BAD16(v) (((v)[1] ^ qhi) | ((v)[3] ^ qhi) | (((v)[0] ^ qlo) >> 16) | (((v)[2] ^ qlo) >> 16))
#define LD16(off) __builtin_amdgcn_raw_buffer_load_b128(rsrc, (off), 0, 16)
#define ST16(v, off) __builtin_amdgcn_raw_buffer_store_b128((v), rsrc, (off), 0, 16)
__device__ __forceinline__ bool timed_out(u64 t0) { return __builtin_amdgcn_s_memrealtime() - t0 > SPIN_TICKS; }
#define STAMP(i) do {} while (0)
#define STAMP_DUMP() do {} while (0)
#define BARRIER() do { asm volatile("s_waitcnt lgkmcnt(0)" ::: "memory"); __builtin_amdgcn_s_barrier(); asm volatile("" ::: "memory"); } while (0)
typedef __attribute__((address_space(3))) unsigned lds_u32;
#define LDS_LD(off) __hip_atomic_load((lds_u32*)(lds + (off)), __ATOMIC_RELAXED, __HIP_MEMORY_SCOPE_WORKGROUP)
#define LDS_ST(off, v) __hip_atomic_store((lds_u32*)(lds + (off)), (v), __ATOMIC_RELAXED, __HIP_MEMORY_SCOPE_WORKGROUP)

__device__ __forceinline__ void ln_stats(u32x4 xv, float& mb, float& sd) {
  float e[8];
#pragma unroll
  for (int i = 0; i < 4; ++i) { e[2 * i] = bf_lo(xv[i]); e[2 * i + 1] = bf_hi(xv[i]); }
  float s = ((e[0] + e[1]) + (e[2] + e[3])) + ((e[4] + e[5]) + (e[6] + e[7]));
  const float mean = wave_sum(s) * (1.0f / DM);
  float q = 0.f;
#pragma unroll
  for (int i = 0; i < 8; ++i) { const float d = e[i] - mean; q = fmaf(d, d, q); }
  const float var = wave_sum(q) * (1.0f / DM);
  mb = rbf(mean);
  sd = rbf(sqrtf(rbf(rbf(var) + 1e-5f)));
}
__device__ __forceinline__ float ln_apply(float x, float mb, float sd) { return rbf(rbf(x - mb) / sd); }

struct Params {
  const unsigned short* x; const unsigned short* k; const unsigned short* v; const unsigned short* wq;
  const unsigned short* wo; const unsigned short* w1; const unsigned short* w2; unsigned short* out; unsigned char* ws;
};

__global__ void __launch_bounds__(NTHR, 1) fused_decode(Params p) {
  __shared__ __attribute__((aligned(16))) unsigned char lds[L_END];
  const int tid = threadIdx.x, lane = tid & 63;
  const int wave = __builtin_amdgcn_readfirstlane(tid >> 6);
  const int bid = blockIdx.x, kvh = bid & 7, r = bid >> 3, b = r >> 4, sp = r & 15;
  unsigned char* ws = p.ws;
  const auto rsrc = __builtin_amdgcn_make_buffer_rsrc(ws, 0, WS_END, 0x00020000);
  const int n = lane & 15, g = lane >> 4;
  const int cg = kvh * 16 + sp;

  float mc = 0.f, l = 0.f;
  f32x4 o0 = {0.f, 0.f, 0.f, 0.f}, o1 = {0.f, 0.f, 0.f, 0.f};
  int dry = 0, tw = wave;

  if (wave < NSW) {
    const size_t kvbase = ((size_t)(b * HKV + kvh) * SK + (size_t)sp * KEYS) * DH;
    const unsigned short* kp = p.k + kvbase + (size_t)(n >> 2) * DH + 8 * (4 * g + (n & 3));
    const unsigned short* vp = p.v + kvbase + 8 * lane;
    const int rot = ((bid * 5 + (bid >> 3) * 11) & 15) * 8;
    u32x4 kr[W][4], vr[W][4];
#pragma unroll
    for (int t = 0; t < W; ++t)
#pragma unroll
      for (int i = 0; i < 4; ++i) { kr[t][i] = u32x4{0u, 0u, 0u, 0u}; vr[t][i] = u32x4{0u, 0u, 0u, 0u}; }
#define STR2(x) #x
#define STR(x) STR2(x)
#define LDKV(dst, ptr, off, n) asm volatile("s_waitcnt vmcnt(" STR(n) ")\n\tglobal_load_dwordx4 %0, %1, off offset:" STR(off) " nt" : "+v"(dst) : "v"(ptr) : "memory")
#define ISSUE(slot, kq_, vq_, n) do { \
      LDKV(kr[slot][0], kq_, 0, n); LDKV(kr[slot][1], kq_, 1024, n); LDKV(kr[slot][2], kq_, 2048, n); LDKV(kr[slot][3], kq_, 3072, n); \
      LDKV(vr[slot][0], vq_, 0, n); LDKV(vr[slot][1], vq_, 1024, n); LDKV(vr[slot][2], vq_, 2048, n); LDKV(vr[slot][3], vq_, 3072, n); \
      __builtin_amdgcn_sched_barrier(0); } while (0)
#define TICKET() ({ unsigned t_ = 0; if (lane == 0) t_ = __hip_atomic_fetch_add((lds_u32*)(lds + L_TICK), 1u, __ATOMIC_RELAXED, __HIP_MEMORY_SCOPE_WORKGROUP); t_; })
    __builtin_amdgcn_s_barrier();
    __builtin_amdgcn_sched_barrier(0);
#pragma unroll
    for (int t = 0; t < W; ++t) {
      const size_t o = (size_t)((t * NSW + wave + rot) & (NTILE - 1)) * (16 * DH);
      const unsigned short* kq = kp + o;
      const unsigned short* vq = vp + o;
      ISSUE(t, kq, vq, INFL_PRO);
    }
    STAMP(0);
    unsigned nxt = TICKET();
    while (LDS_LD(L_QRDY) == 0u) __builtin_amdgcn_s_sleep(1);
    asm volatile("" ::: "memory");
    const s16x8 qf = *reinterpret_cast<const s16x8*>(lds + L_QF + ((n >> 2) * 16 + 4 * g + (n & 3)) * 16);
    const unsigned jm = n & 3;
    const unsigned mask0 = jm == 0 ? 0x0000ffffu : (jm == 1 ? 0xffff0000u : 0u);
    const unsigned mask1 = jm == 2 ? 0x0000ffffu : (jm == 3 ? 0xffff0000u : 0u);
    const bool sel1 = (lane & 1) != 0, sel2 = (lane & 2) != 0;
    float m = -INFINITY;
    STAMP(1);

#define TILE(slot) do { \
      asm volatile("" : "+v"(kr[slot][0]), "+v"(kr[slot][1]), "+v"(kr[slot][2]), "+v"(kr[slot][3]), \
                        "+v"(vr[slot][0]), "+v"(vr[slot][1]), "+v"(vr[slot][2]), "+v"(vr[slot][3])); \
      float s_[4]; \
      _Pragma("unroll") for (int i_ = 0; i_ < 4; ++i_) { \
        const f32x4 d_ = __builtin_amdgcn_mfma_f32_16x16x32_bf16(__builtin_bit_cast(s16x8, kr[slot][i_]), qf, f32x4{0.f, 0.f, 0.f, 0.f}, 0, 0, 0); \
        const float t0_ = sel1 ? d_[1] : d_[0], t1_ = sel1 ? d_[3] : d_[2]; \
        float u_ = sel2 ? t1_ : t0_; u_ += dpp<XOR1>(u_); u_ += dpp<XOR2>(u_); s_[i_] = u_; } \
      const float mx_ = fmaxf(fmaxf(s_[0], s_[1]), fmaxf(s_[2], s_[3])); \
      if (__builtin_amdgcn_ballot_w64(mx_ > m) != 0) { \
        const float mn_ = fmaxf(m, max_x16_x32(mx_)); \
        const float mcn_ = mn_ * C2; \
        const float al_ = (m == -INFINITY) ? 0.f : __builtin_amdgcn_exp2f(mc - mcn_); \
        l *= al_; m = mn_; mc = mcn_; \
        const float a0_ = asf(__builtin_amdgcn_readlane((int)asu(al_), 0)); \
        const float a1_ = asf(__builtin_amdgcn_readlane((int)asu(al_), 4)); \
        const float a2_ = asf(__builtin_amdgcn_readlane((int)asu(al_), 8)); \
        const float a3_ = asf(__builtin_amdgcn_readlane((int)asu(al_), 12)); \
        const float ao_ = g == 0 ? a0_ : (g == 1 ? a1_ : (g == 2 ? a2_ : a3_)); \
        o0 *= ao_; o1 *= ao_; } \
      _Pragma("unroll") for (int i_ = 0; i_ < 4; ++i_) { \
        const float p_ = __builtin_amdgcn_exp2f(fmaf(s_[i_], C2, -mc)); \
        l += p_; \
        const unsigned pb_ = cvtpk(p_, p_); \
        const u32x2 a_ = {pb_ & mask0, pb_ & mask1}; \
        const u32x2 va_ = {vr[slot][i_][0], vr[slot][i_][1]}, vb_ = {vr[slot][i_][2], vr[slot][i_][3]}; \
        o0 = __builtin_amdgcn_mfma_f32_16x16x16bf16_1k(__builtin_bit_cast(s16x4, a_), __builtin_bit_cast(s16x4, va_), o0, 0, 0, 0); \
        o1 = __builtin_amdgcn_mfma_f32_16x16x16bf16_1k(__builtin_bit_cast(s16x4, a_), __builtin_bit_cast(s16x4, vb_), o1, 0, 0, 0); } \
      asm volatile("" : "+v"(o0), "+v"(o1)); \
    } while (0)

    int left = W;
#pragma unroll 1
    for (;;) {
#pragma unroll
      for (int s = 0; s < W; ++s) {
        TILE(s);
        __builtin_amdgcn_sched_barrier(0);
        const unsigned cur = __builtin_amdgcn_readfirstlane(nxt);
        if (cur < (unsigned)NTILE) {
          nxt = TICKET();
          if (cur == (unsigned)(NTILE - 36) && lane == 0) LDS_ST(L_PROG, 1u);
          const size_t o = (size_t)((cur + rot) & (NTILE - 1)) * (16 * DH);
          const unsigned short* kq = kp + o;
          const unsigned short* vq = vp + o;
          ISSUE(s, kq, vq, INFL_MAIN);
        } else {
          if (--left == 0) goto stream_done;
          if (left == 1) asm volatile("s_waitcnt vmcnt(0)" ::: "memory");
          __builtin_amdgcn_sched_barrier(0);
        }
      }
    }
  stream_done:
    ;
#undef TILE
#undef ISSUE
#undef LDKV
#undef TICKET
  } else {
    __builtin_amdgcn_s_setprio(3);
    const int qw = wave - NSW;
    STAMP(0);
    const int row0 = qw * 128 + (lane >> 1);
    u32x4 wqv[QR];
    {
      const unsigned short* wp = p.wq + (size_t)row0 * 4096 + kvh * 512 + r * 16 + 8 * (lane & 1);
#pragma unroll
      for (int i = 0; i < QR; ++i) wqv[i] = ldnt(wp + (size_t)i * 32 * 4096);
    }
    const u32x4 xa = *reinterpret_cast<const u32x4*>(p.x + 8 * lane);
    const u32x4 xb = *reinterpret_cast<const u32x4*>(p.x + DM + 8 * lane);
    unsigned short xs[QR];
#pragma unroll
    for (int i = 0; i < QR; ++i) xs[i] = p.x[(lane & 1) * DM + row0 + 32 * i];
    unsigned short xh[4];
#pragma unroll
    for (int i = 0; i < 2; ++i) { xh[2 * i] = p.x[qw * 128 + 64 * i + lane]; xh[2 * i + 1] = p.x[DM + qw * 128 + 64 * i + lane]; }
    u64 ep = 0; unsigned xo = 0;
    if (qw == 0) {
      ep = ld_g(ws, WS_E) + 1ull;
      xo = *reinterpret_cast<const unsigned*>(p.x + b * DM + 4 * cg + 2 * (lane & 1));
      if (lane == 0) { LDS_ST(L_QCNT, 0u); LDS_ST(L_QRDY, 0u); LDS_ST(L_PROG, 0u); LDS_ST(L_TICK, (unsigned)(W * NSW)); LDS_ST(L_TRDY, 0u); }
    }
    u32x4 w1v[2];
#pragma unroll
    for (int i = 0; i < 2; ++i) w1v[i] = ldnt(p.w1 + (size_t)(qw * 128 + 64 * i + lane) * DFF + 224 * kvh + 8 * (r < 28 ? r : 0));
    __builtin_amdgcn_sched_barrier(0);
    u32x4 wst[11], w2v[7];
    if (qw != 0) {
      const unsigned short* wp = p.wo + (size_t)(kvh * 512 + (sp >> 2) * 128 + g) * DM + (sp & 3) * 128 + 8 * n;
#pragma unroll
      for (int i = 0; i < 11; ++i) { const int q4 = min((qw - 1) * 11 + i, 31); wst[i] = ldnt(wp + (size_t)q4 * 4 * DM); }
    }
    if (qw == 3) {
      const unsigned short* wp = p.w2 + (size_t)(224 * kvh + (lane >> 1)) * DM + 16 * r + 8 * (lane & 1);
#pragma unroll
      for (int i = 0; i < 7; ++i) w2v[i] = ldnt(wp + (size_t)i * 32 * DM);
    }
    __builtin_amdgcn_sched_barrier(0);
    BARRIER();
    {
      float m0, s0, m1, s1;
      ln_stats(xa, m0, s0); ln_stats(xb, m1, s1);
      const float mb = (lane & 1) ? m1 : m0, sd = (lane & 1) ? s1 : s0;
      float qa[16];
#pragma unroll
      for (int c = 0; c < 16; ++c) qa[c] = 0.f;
#pragma unroll
      for (int i = 0; i < QR; ++i) {
        const float a = ln_apply(bf_lo(xs[i]), mb, sd);
        const float ao = dpp<XOR1>(a);
        const float a0 = (lane & 1) ? ao : a, a1 = (lane & 1) ? a : ao;
#pragma unroll
        for (int c = 0; c < 4; ++c) {
          const float lo = bf_lo(wqv[i][c]), hi = bf_hi(wqv[i][c]);
          qa[2 * c] = fmaf(a0, lo, qa[2 * c]); qa[2 * c + 1] = fmaf(a0, hi, qa[2 * c + 1]);
          qa[8 + 2 * c] = fmaf(a1, lo, qa[8 + 2 * c]); qa[8 + 2 * c + 1] = fmaf(a1, hi, qa[8 + 2 * c + 1]);
        }
      }
#pragma unroll
      for (int i = 0; i < 8; ++i) qa[i] = red32(qa[i], qa[i + 8]);
#pragma unroll
      for (int i = 0; i < 4; ++i) qa[i] = red16(qa[i], qa[i + 4]);
#pragma unroll
      for (int i = 0; i < 4; ++i) qa[i] = row_sum8_odd_even(qa[i]);
      if ((lane & 14) == 0)
        *reinterpret_cast<f32x4*>(lds + L_Q + qw * 128 + (lane >> 5) * 64 + (8 * (lane & 1) + 4 * ((lane >> 4) & 1)) * 4) = f32x4{qa[0], qa[1], qa[2], qa[3]};
      asm volatile("s_waitcnt lgkmcnt(0)" ::: "memory");
      if (lane == 0) __hip_atomic_fetch_add((lds_u32*)(lds + L_QCNT), 1u, __ATOMIC_RELAXED, __HIP_MEMORY_SCOPE_WORKGROUP);
      if (qw == 0) {
        const unsigned qhi = (unsigned)(ep >> 16), qlo = ((unsigned)ep) << 16;
        if (lane == 0) { LDS_ST(L_TAG, ((unsigned)ep) << 24); LDS_ST(L_TAG + 4, (unsigned)(ep >> 8)); LDS_ST(L_TAG + 8, qlo); LDS_ST(L_TAG + 12, qhi); }
        asm volatile("s_waitcnt lgkmcnt(0)" ::: "memory");
        if (lane == 0) LDS_ST(L_TRDY, 1u);
        while ((LDS_LD(L_QCNT) & 0xffffu) != (unsigned)NQW) { }
        asm volatile("" ::: "memory");
        STAMP(1);
        float qs = 0.f;
#pragma unroll
        for (int w = 0; w < NQW; ++w) qs += *reinterpret_cast<const float*>(lds + L_Q + w * 128 + (lane & 31) * 4);
        if (lane < 32)
          st_g(ws, WS_Q + (((lane >> 4) * 8 + kvh) * 512 + r * 16 + (lane & 15)) * 8, (ep << 16) | (cvtpk(qs, qs) & 0xffffu));
      }
      float ha[16];
#pragma unroll
      for (int c = 0; c < 16; ++c) ha[c] = 0.f;
#pragma unroll
      for (int i = 0; i < 2; ++i) {
        const float a0 = ln_apply(bf_lo(xh[2 * i]), m0, s0), a1 = ln_apply(bf_lo(xh[2 * i + 1]), m1, s1);
#pragma unroll
        for (int c = 0; c < 4; ++c) {
          const float lo = bf_lo(w1v[i][c]), hi = bf_hi(w1v[i][c]);
          ha[2 * c] = fmaf(a0, lo, ha[2 * c]); ha[2 * c + 1] = fmaf(a0, hi, ha[2 * c + 1]);
          ha[8 + 2 * c] = fmaf(a1, lo, ha[8 + 2 * c]); ha[8 + 2 * c + 1] = fmaf(a1, hi, ha[8 + 2 * c + 1]);
        }
      }
#pragma unroll
      for (int i = 0; i < 8; ++i) ha[i] = red32(ha[i], ha[i + 8]);
#pragma unroll
      for (int i = 0; i < 4; ++i) ha[i] = red16(ha[i], ha[i + 4]);
#pragma unroll
      for (int i = 0; i < 4; ++i) ha[i] = row_sum16(ha[i]);
      if ((lane & 15) == 0) *reinterpret_cast<f32x4*>(lds + L_HP + qw * 64 + g * 16) = f32x4{ha[0], ha[1], ha[2], ha[3]};
      asm volatile("s_waitcnt lgkmcnt(0)" ::: "memory");
      if (lane == 0) __hip_atomic_fetch_add((lds_u32*)(lds + L_QCNT), 0x10000u, __ATOMIC_RELAXED, __HIP_MEMORY_SCOPE_WORKGROUP);
    }
    if (qw != 0) {
#pragma unroll
      for (int i = 0; i < 11; ++i) { const int q4 = min((qw - 1) * 11 + i, 31); *reinterpret_cast<u32x4*>(lds + L_WO + (q4 * 4 + g) * 256 + ((n ^ (2 * (q4 & 7))) * 16)) = wst[i]; }
      asm volatile("s_waitcnt lgkmcnt(0)" ::: "memory");
      if (qw == 2) return;
      if (qw == 1) { dry = 1; tw = 0; goto tail; }
      while ((LDS_LD(L_QCNT) >> 16) != (unsigned)NQW || LDS_LD(L_TRDY) == 0u) { }
      asm volatile("" ::: "memory");
      const unsigned tlo = LDS_LD(L_TAG), thi = LDS_LD(L_TAG + 4), qlo = LDS_LD(L_TAG + 8), qhi = LDS_LD(L_TAG + 12);
      {
        float hs = 0.f;
#pragma unroll
        for (int w = 0; w < NQW; ++w) hs += *reinterpret_cast<const float*>(lds + L_HP + w * 64 + n * 4);
        const float hr = fmaxf(rbf(hs), 0.f);
        if (r < 28 && lane < 16) st_g(ws, WS_HX + ((kvh * 32 + r) * 16 + lane) * 8, ((u64)qhi << 32) | qlo | (asu(hr) >> 16));
      }
      {
        const unsigned hb = WS_HX + kvh * 4096 + lane * 16;
        const u64 t0 = __builtin_amdgcn_s_memrealtime();
        u32x4 hv[4];
        for (;;) {
          __builtin_amdgcn_s_sleep(64);
          asm volatile("" ::: "memory");
#pragma unroll
          for (int i = 0; i < 4; ++i) hv[i] = LD16(hb + i * 1024);
          const unsigned bad = BAD16(hv[0]) | BAD16(hv[1]) | BAD16(hv[2]) | (lane < 32 ? BAD16(hv[3]) : 0u);
          if (__builtin_amdgcn_ballot_w64(bad != 0) == 0 || timed_out(t0)) break;
        }
#pragma unroll
        for (int i = 0; i < 4; ++i) {
          const int e = 64 * i + lane;
          *reinterpret_cast<f32x2*>(lds + L_HS + ((e >> 2) & 1) * 1024 + ((e >> 3) * 8 + 2 * (e & 3)) * 4) = f32x2{bf_lo(hv[i][0]), bf_lo(hv[i][2])};
        }
        asm volatile("s_waitcnt lgkmcnt(0)" ::: "memory");
        float ya[16];
#pragma unroll
        for (int c = 0; c < 16; ++c) ya[c] = 0.f;
#pragma unroll
        for (int i = 0; i < 7; ++i) {
          const float a0 = *reinterpret_cast<const float*>(lds + L_HS + (32 * i + (lane >> 1)) * 4);
          const float a1 = *reinterpret_cast<const float*>(lds + L_HS + 1024 + (32 * i + (lane >> 1)) * 4);
#pragma unroll
          for (int c = 0; c < 4; ++c) {
            const float lo = bf_lo(w2v[i][c]), hi = bf_hi(w2v[i][c]);
            ya[2 * c] = fmaf(a0, lo, ya[2 * c]); ya[2 * c + 1] = fmaf(a0, hi, ya[2 * c + 1]);
            ya[8 + 2 * c] = fmaf(a1, lo, ya[8 + 2 * c]); ya[8 + 2 * c + 1] = fmaf(a1, hi, ya[8 + 2 * c + 1]);
          }
        }
#pragma unroll
        for (int i = 0; i < 8; ++i) ya[i] = red32(ya[i], ya[i + 8]);
#pragma unroll
        for (int i = 0; i < 4; ++i) ya[i] = red16(ya[i], ya[i + 4]);
#pragma unroll
        for (int i = 0; i < 4; ++i) ya[i] = row_sum8_odd_even(ya[i]);
        const float y0 = (lane & 2) ? ya[2] : ya[0], y1 = (lane & 2) ? ya[3] : ya[1];
        if ((lane & 12) == 0)
          ST16((u32x4{f24(y0, tlo), thi, f24(y1, tlo), thi}),
               WS_YX + ((kvh * 32 + r) * 32 + (lane >> 5) * 16 + (lane & 1) * 8 + ((lane >> 4) & 1) * 4 + (lane & 2)) * 8);
      }
      STAMP(9);
      while (LDS_LD(L_PROG) == 0u) __builtin_amdgcn_s_sleep(32);
      STAMP(10);
      {
        const unsigned yb = WS_YX + (((lane >> 1) & 7) * 32 + (cg >> 2)) * 256 + (b * 16 + (cg & 3) * 4 + 2 * (lane & 1)) * 8;
        const u64 t0 = __builtin_amdgcn_s_memrealtime();
        u32x4 yv;
        for (;;) {
          asm volatile("" ::: "memory");
          yv = LD16(yb);
          if (__builtin_amdgcn_ballot_w64(BAD24(yv) != 0) == 0 || timed_out(t0)) break;
          __builtin_amdgcn_s_sleep(32);
        }
        const float ya = rbf(row_sum8_odd_even(uf24(yv[0]))), yc = rbf(row_sum8_odd_even(uf24(yv[2])));
        if (lane < 2) *reinterpret_cast<f32x2*>(lds + L_GATE + lane * 8) = f32x2{rbf(ya / (1.f + __expf(-ya))), rbf(yc / (1.f + __expf(-yc)))};
        asm volatile("s_waitcnt lgkmcnt(0)" ::: "memory");
      }
      STAMP(5);
      STAMP_DUMP();
      return;
    }
    const unsigned qhi = (unsigned)(ep >> 16), qlo = ((unsigned)ep) << 16;
    const unsigned thi = (unsigned)(ep >> 8), tlo = ((unsigned)ep) << 24;
    {
      const unsigned qb = WS_Q + (b * 8 + kvh) * 4096 + lane * 64;
      const u64 t0 = __builtin_amdgcn_s_memrealtime();
      u32x4 qv[4];
      for (;;) {
        asm volatile("" ::: "memory");
#pragma unroll
        for (int i = 0; i < 4; ++i) qv[i] = LD16(qb + i * 16);
        unsigned bad = 0;
#pragma unroll
        for (int i = 0; i < 4; ++i) bad |= BAD16(qv[i]);
        if (__builtin_amdgcn_ballot_w64(bad != 0) == 0 || timed_out(t0)) break;
      }
      u32x4 f;
#pragma unroll
      for (int i = 0; i < 4; ++i) f[i] = (qv[i][0] & 0xffffu) | (qv[i][2] << 16);
      *reinterpret_cast<u32x4*>(lds + L_QF + lane * 16) = f;
      asm volatile("s_waitcnt lgkmcnt(0)" ::: "memory");
      if (lane == 0) LDS_ST(L_QRDY, 1u);
    }
    STAMP(2);
    BARRIER();
    STAMP(6);
    BARRIER();
    STAMP(11);
    {
      const f32x2 gate = *reinterpret_cast<const f32x2*>(lds + L_GATE + (lane & 1) * 8);
      const unsigned ib = WS_H2 + (((b * 32 + (cg >> 2)) * 32 + (lane >> 1)) * 16 + (cg & 3) * 4 + 2 * (lane & 1)) * 8;
      const u64 t0 = __builtin_amdgcn_s_memrealtime();
      u32x4 sv = {0u, ~thi, 0u, 0u};
      for (;;) {
        asm volatile("" ::: "memory");
        if (BAD24(sv) != 0) sv = LD16(ib);
        if (__builtin_amdgcn_ballot_w64(BAD24(sv) != 0) == 0 || timed_out(t0)) break;
      }
      float s0 = row_sum8_odd_even(uf24(sv[0])); s0 = red16(s0, s0); s0 = red32(s0, s0);
      float s1 = row_sum8_odd_even(uf24(sv[2])); s1 = red16(s1, s1); s1 = red32(s1, s1);
      const float r0 = bf_lo(xo) + rbf(rbf(s0) * gate[0]), r1 = bf_hi(xo) + rbf(rbf(s1) * gate[1]);
      if (lane < 2) *reinterpret_cast<unsigned*>(p.out + b * DM + 4 * cg + 2 * lane) = cvtpk(r0, r1);
      if (bid == 0 && lane == 0) st_g(ws, WS_E, ep);
      STAMP(8);
      STAMP_DUMP();
    }
    return;
  }
tail:
  asm volatile("" : "+s"(dry));
  {
    l = red16(l, l); l = red32(l, l);
    if (!dry) {
      *reinterpret_cast<f32x4*>(lds + L_O + tw * 2048 + g * 512 + n * 32) = o0;
      *reinterpret_cast<f32x4*>(lds + L_O + tw * 2048 + g * 512 + n * 32 + 16) = o1;
    }
    if (!dry && (lane & 0x33) == 0) {
      *reinterpret_cast<float*>(lds + L_M + tw * 16 + (n >> 2) * 4) = mc;
      *reinterpret_cast<float*>(lds + L_L + tw * 16 + (n >> 2) * 4) = l;
    }
    const unsigned tlo = LDS_LD(L_TAG), thi = LDS_LD(L_TAG + 4);
    STAMP(5);
    if (!dry) BARRIER();
    STAMP(6);
    if (tw < 4) {
      float mw[NSW], M = -INFINITY;
#pragma unroll
      for (int w = 0; w < NSW; ++w) { mw[w] = *reinterpret_cast<const float*>(lds + L_M + w * 16 + tw * 4); M = fmaxf(M, mw[w]); }
      M = asf(asu(M) & 0xffffff00u);
      float Oa = 0.f, Ob = 0.f, L = 0.f;
#pragma unroll
      for (int w = 0; w < NSW; ++w) {
        const float wt = __builtin_amdgcn_exp2f(mw[w] - M);
        const f32x2 ov = *reinterpret_cast<const f32x2*>(lds + L_O + w * 2048 + tw * 512 + lane * 8);
        Oa = fmaf(wt, ov[0], Oa); Ob = fmaf(wt, ov[1], Ob);
        L = fmaf(wt, *reinterpret_cast<const float*>(lds + L_L + w * 16 + tw * 4), L);
      }
      if (!dry) ST16((u32x4{f24(Oa, tlo), thi, f24(Ob, tlo), thi}), WS_H1 + bid * 8192 + tw * 1152 + lane * 16);
      if (!dry && lane == 0) ST16((u32x4{f24(M, tlo), thi, f24(L, tlo), thi}), WS_H1 + bid * 8192 + tw * 1152 + 1024);
    }
    STAMP(7);
    f32x2 wf[4][4];
#pragma unroll
    for (int i = 0; i < 4; ++i) {
      const u32x4 wv = *reinterpret_cast<const u32x4*>(lds + L_WO + (4 * (lane >> 1) + i) * 256 + (((2 * tw + (lane & 1)) ^ (lane & 14)) * 16));
#pragma unroll
      for (int c = 0; c < 4; ++c) { wf[i][c] = f32x2{bf_lo(wv[c]), bf_hi(wv[c])}; asm volatile("" : "+v"(wf[i][c])); }
    }
    const int jh = sp >> 2, jc = sp & 3;
    {
      const unsigned gb = WS_H1 + (unsigned)(kvh + 128 * b) * 8192u;
      const unsigned oa_off = gb + (2 * tw) * 65536 + jh * 1152 + lane * 16, ml_off = gb + (2 * tw + (lane & 1)) * 65536 + jh * 1152 + 1024;
      const u64 t0 = __builtin_amdgcn_s_memrealtime();
      u32x4 va = {0u, ~thi, 0u, 0u}, vb = va, vm = va;
      for (;;) {
        asm volatile("" ::: "memory");
        if (BAD24(va) != 0) va = LD16(oa_off);
        if (BAD24(vb) != 0) vb = LD16(oa_off + 65536);
        if (BAD24(vm) != 0) vm = LD16(ml_off);
        if (__builtin_amdgcn_ballot_w64((BAD24(va) | BAD24(vb) | BAD24(vm)) != 0) == 0 || dry || timed_out(t0)) break;
      }
      const float Ms = uf24(vm[0]), M = fmaxf(Ms, dpp<XOR1>(Ms));
      const float wt = __builtin_amdgcn_exp2f(Ms - M), wo_ = dpp<XOR1>(wt);
      const float wl = wt * uf24(vm[2]), L = wl + dpp<XOR1>(wl);
      const float wa = (lane & 1) ? wo_ : wt, wb = (lane & 1) ? wt : wo_;
      if (!dry) *reinterpret_cast<f32x2*>(lds + L_OP + tw * 512 + lane * 8) = f32x2{fmaf(wa, uf24(va[0]), wb * uf24(vb[0])), fmaf(wa, uf24(va[2]), wb * uf24(vb[2]))};
      if (!dry && lane == 0) *reinterpret_cast<f32x2*>(lds + L_ML2 + tw * 8) = f32x2{M, L};
    }
    STAMP(4);
    if (!dry) BARRIER();
    STAMP(8);
    {
      f32x4 ml[NSW / 2], op[NSW];
#pragma unroll
      for (int w = 0; w < NSW / 2; ++w) ml[w] = *reinterpret_cast<const f32x4*>(lds + L_ML2 + w * 16);
#pragma unroll
      for (int w = 0; w < NSW; ++w) op[w] = *reinterpret_cast<const f32x4*>(lds + L_OP + w * 512 + (lane >> 1) * 16);
      __builtin_amdgcn_sched_barrier(0);
      float M = -INFINITY, L = 0.f, cw[NSW];
#pragma unroll
      for (int w = 0; w < NSW / 2; ++w) M = fmaxf(M, fmaxf(ml[w][0], ml[w][2]));
#pragma unroll
      for (int w = 0; w < NSW / 2; ++w) {
        cw[2 * w] = __builtin_amdgcn_exp2f(ml[w][0] - M); cw[2 * w + 1] = __builtin_amdgcn_exp2f(ml[w][2] - M);
        L = fmaf(cw[2 * w], ml[w][1], L); L = fmaf(cw[2 * w + 1], ml[w][3], L);
      }
      f32x4 ov = cw[0] * op[0];
#pragma unroll
      for (int w = 1; w < NSW; ++w) ov += cw[w] * op[w];
      float inv = __builtin_amdgcn_rcpf(L);
      inv = fmaf(fmaf(-L, inv, 1.0f), inv, inv);
      f32x2 ac[4];
#pragma unroll
      for (int i = 0; i < 4; ++i) {
        const float o = rbf(ov[i] * inv);
#pragma unroll
        for (int c = 0; c < 4; ++c) ac[c] = i == 0 ? o * wf[0][c] : ac[c] + o * wf[i][c];
      }
      float acc[4];
      acc[0] = red32(ac[0][0], ac[2][0]); acc[1] = red32(ac[0][1], ac[2][1]);
      acc[2] = red32(ac[1][0], ac[3][0]); acc[3] = red32(ac[1][1], ac[3][1]);
#pragma unroll
      for (int c = 0; c < 2; ++c) acc[c] = red16(acc[c], acc[c + 2]);
      const float e0 = row_sum8_odd_even(acc[0]), e1 = row_sum8_odd_even(acc[1]);
      if (!dry && (lane & 14) == 0)
        ST16((u32x4{f24(e0, tlo), thi, f24(e1, tlo), thi}),
             WS_H2 + (((b * 32 + jc * 8 + tw) * 32 + kvh * 4 + jh) * 16 + 8 * (lane & 1) + 4 * (lane >> 5) + 2 * ((lane >> 4) & 1)) * 8);
    }
    STAMP(9);
    if (!dry) STAMP_DUMP();
  }
}

}

extern "C" void kernel_launch(void* const* d_in, const int* in_sizes, int n_in, void* d_out, int out_size, void* d_ws,
                              size_t ws_size, hipStream_t stream) {
  Params p;
  p.x = (const unsigned short*)d_in[0]; p.k = (const unsigned short*)d_in[1]; p.v = (const unsigned short*)d_in[2];
  p.wq = (const unsigned short*)d_in[3]; p.wo = (const unsigned short*)d_in[4];
  p.w1 = (const unsigned short*)d_in[6]; p.w2 = (const unsigned short*)d_in[7];
  p.out = (unsigned short*)d_out; p.ws = (unsigned char*)d_ws;
  hipLaunchKernelGGL(fused_decode, dim3(256), dim3(NTHR), 0, stream, p);
}
```

```cpp
#include <hip/hip_runtime.h>
#include <hip/hip_bf16.h>
#include <cstdint>

namespace {

typedef unsigned u32x4 __attribute__((ext_vector_type(4)));
typedef unsigned u32x2 __attribute__((ext_vector_type(2)));
typedef float f32x4 __attribute__((ext_vector_type(4)));
typedef float f32x2 __attribute__((ext_vector_type(2)));
typedef __bf16 bf16x2 __attribute__((ext_vector_type(2)));
typedef short s16x8 __attribute__((ext_vector_type(8)));
typedef short s16x4 __attribute__((ext_vector_type(4)));
typedef unsigned long long u64;
#define GAS __attribute__((address_space(1)))
typedef GAS u64 gu64;
typedef const GAS unsigned short* gptr;

constexpr int SK = 32768, DH = 128, DM = 512, DFF = 1792, HKV = 8;
constexpr int NSPLIT = 16, KEYS = SK / NSPLIT;
constexpr int NSW = 8;
constexpr int NQW = 4;
#define INFL_PRO 3
#define INFL_LO2 1
#define INFL_LO 2
#define INFL_MAIN 4
#define INFL_HI 7
constexpr int CTRL_T0 = 350, CTRL_TR = 227;
constexpr int CTRL_BAND = 40, CTRL_BAND2 = 220;
constexpr int QDELAY = 600;
constexpr int NTHR = (NSW + NQW) * 64;
constexpr int NTILE = KEYS / 16;
constexpr int W = 4;

constexpr unsigned WS_E = 0;
constexpr unsigned WS_FLAG = 0x1000;
constexpr unsigned WS_Q = 0x2000;
constexpr unsigned WS_HX = 0x12000;
constexpr unsigned WS_YX = 0x20000;
constexpr unsigned OBX = 4608;
constexpr unsigned WS_H2 = 0x30000;
constexpr unsigned WS_H1 = 0x70000;
constexpr unsigned WS_END = WS_H1 + 256 * OBX;

constexpr float C2 = 0.08838834764831845f * 1.4426950408889634f;
constexpr u64 SPIN_TICKS = 4000000ull;

constexpr int L_O = 0;
constexpr int L_M = L_O + NSW * 2048;
constexpr int L_L = L_M + NSW * 16;
constexpr int L_Q = L_L + NSW * 16;
constexpr int L_XN = L_Q + 4 * 512;
constexpr int L_QF = L_XN + 4096;
constexpr int L_OP = L_QF + 1024;
constexpr int L_ML2 = L_OP + NSW * 512;
constexpr int L_WO = L_ML2 + NSW * 8;
constexpr int L_TAG = L_WO + 128 * 256;
constexpr int L_GATE = L_TAG + 32;
constexpr int L_QCNT = L_GATE + 16;
constexpr int L_QRDY = L_QCNT + 4;
constexpr int L_PROG = L_QCNT + 8;
constexpr int L_TRDY = L_QCNT + 12;
constexpr int L_END = L_QCNT + 16;

__device__ __forceinline__ float asf(unsigned u) { return __builtin_bit_cast(float, u); }
__device__ __forceinline__ unsigned asu(float f) { return __builtin_bit_cast(unsigned, f); }
__device__ __forceinline__ float bf_lo(unsigned u) { return asf(u << 16); }
__device__ __forceinline__ float bf_hi(unsigned u) { return asf(u & 0xffff0000u); }
__device__ __forceinline__ unsigned cvtpk(float lo, float hi) {
  const f32x2 v = {lo, hi};
  return __builtin_bit_cast(unsigned, __builtin_convertvector(v, bf16x2));
}
__device__ __forceinline__ float rbf(float x) { return bf_lo(cvtpk(x, x)); }
template <int CTRL> __device__ __forceinline__ float dpp(float x) {
  return asf(__builtin_amdgcn_mov_dpp((int)asu(x), CTRL, 0xf, 0xf, true));
}
constexpr int XOR1 = 0xB1, XOR2 = 0x4E, HMIRR = 0x141, MIRR = 0x140, ROR4 = 0x124, ROR8 = 0x128;
__device__ __forceinline__ float red32(float a, float b) {
  auto r = __builtin_amdgcn_permlane32_swap(asu(a), asu(b), false, false);
  return asf((unsigned)r[0]) + asf((unsigned)r[1]);
}
__device__ __forceinline__ float red16(float a, float b) {
  auto r = __builtin_amdgcn_permlane16_swap(asu(a), asu(b), false, false);
  return asf((unsigned)r[0]) + asf((unsigned)r[1]);
}
__device__ __forceinline__ float max_x16_x32(float x) {
  auto s = __builtin_amdgcn_permlane16_swap(asu(x), asu(x), false, false);
  x = fmaxf(asf((unsigned)s[0]), asf((unsigned)s[1]));
  auto t = __builtin_amdgcn_permlane32_swap(asu(x), asu(x), false, false);
  return fmaxf(asf((unsigned)t[0]), asf((unsigned)t[1]));
}
__device__ __forceinline__ float row_sum16(float x) {
  x += dpp<XOR1>(x); x += dpp<XOR2>(x); x += dpp<HMIRR>(x); x += dpp<MIRR>(x); return x;
}
__device__ __forceinline__ float row_sum8_odd_even(float t) {
  t += dpp<ROR8>(t); t += dpp<ROR4>(t); t += dpp<XOR2>(t); return t;
}
__device__ __forceinline__ float wave_sum(float x) { x = row_sum16(x); x = red16(x, x); return red32(x, x); }
__device__ __forceinline__ u32x4 ldnt(gptr p) { return __builtin_nontemporal_load((const GAS u32x4*)p); }
__device__ __forceinline__ u32x4 ld16(gptr p) { return *(const GAS u32x4*)p; }
__device__ __forceinline__ void st_g(unsigned char* ws, unsigned off, u64 v) {
  __hip_atomic_store((gu64*)(ws + off), v, __ATOMIC_RELAXED, __HIP_MEMORY_SCOPE_AGENT);
}
__device__ __forceinline__ u64 ld_g(unsigned char* ws, unsigned off) {
  return __hip_atomic_load((gu64*)(ws + off), __ATOMIC_RELAXED, __HIP_MEMORY_SCOPE_AGENT);
}
__device__ __forceinline__ unsigned f24(float x, unsigned tlo) { const unsigned u = asu(x); return ((u + 0x7fu + ((u >> 8) & 1u)) >> 8) | tlo; }
__device__ __forceinline__ float uf24(unsigned lo) { return asf(lo << 8); }
#define BAD24(v) (((v)[1] ^ thi) | ((v)[3] ^ thi) | (((v)[0] ^ tlo) >> 24) | (((v)[2] ^ tlo) >> 24))
#define BAD16(v) (((v)[1] ^ qhi) | ((v)[3] ^ qhi) | (((v)[0] ^ qlo) >> 16) | (((v)[2] ^ qlo) >> 16))
#define LD16(off) __builtin_amdgcn_raw_buffer_load_b128(rsrc, (off), 0, 16)
#define ST16(v, off) __builtin_amdgcn_raw_buffer_store_b128((v), rsrc, (off), 0, 16)
__device__ __forceinline__ bool timed_out(u64 t0) { return __builtin_amdgcn_s_memrealtime() - t0 > SPIN_TICKS; }
#define BARRIER() do { asm volatile("s_waitcnt lgkmcnt(0)" ::: "memory"); __builtin_amdgcn_s_barrier(); asm volatile("" ::: "memory"); } while (0)
typedef __attribute__((address_space(3))) unsigned lds_u32;
#define LDS_LD(off) __hip_atomic_load((lds_u32*)(lds + (off)), __ATOMIC_RELAXED, __HIP_MEMORY_SCOPE_WORKGROUP)
#define LDS_ST(off, v) __hip_atomic_store((lds_u32*)(lds + (off)), (v), __ATOMIC_RELAXED, __HIP_MEMORY_SCOPE_WORKGROUP)

__device__ __forceinline__ void ln_stats(u32x4 xv, float& mb, float& sd) {
  float e[8];
#pragma unroll
  for (int i = 0; i < 4; ++i) { e[2 * i] = bf_lo(xv[i]); e[2 * i + 1] = bf_hi(xv[i]); }
  float s = ((e[0] + e[1]) + (e[2] + e[3])) + ((e[4] + e[5]) + (e[6] + e[7]));
  const float mean = wave_sum(s) * (1.0f / DM);
  float q = 0.f;
#pragma unroll
  for (int i = 0; i < 8; ++i) { const float d = e[i] - mean; q = fmaf(d, d, q); }
  const float var = wave_sum(q) * (1.0f / DM);
  mb = rbf(mean);
  sd = rbf(sqrtf(rbf(rbf(var) + 1e-5f)));
}
__device__ __forceinline__ float ln_apply(float x, float mb, float sd) { return rbf(rbf(x - mb) / sd); }

struct Params {
  const unsigned short* x; const unsigned short* k; const unsigned short* v; const unsigned short* wq;
  const unsigned short* wo; const unsigned short* w1; const unsigned short* w2; unsigned short* out; unsigned char* ws;
};

__global__ void __launch_bounds__(NTHR, 3) fused_decode(Params p) {
  __shared__ __attribute__((aligned(16))) unsigned char lds[L_END];
  asm volatile("" : "+s"(p.x), "+s"(p.k), "+s"(p.v), "+s"(p.wq), "+s"(p.wo), "+s"(p.w1), "+s"(p.w2), "+s"(p.out), "+s"(p.ws));
  const gptr gx = (gptr)p.x, gk = (gptr)p.k, gv = (gptr)p.v, gwq = (gptr)p.wq, gwo = (gptr)p.wo, gw1 = (gptr)p.w1, gw2 = (gptr)p.w2;
  const int tid = threadIdx.x, lane = tid & 63;
  const int wave = __builtin_amdgcn_readfirstlane(tid >> 6);
  const int bid = blockIdx.x, kvh = bid & 7, r = bid >> 3, b = r >> 4, sp = r & 15;
  unsigned char* ws = p.ws;
  const auto rsrc = __builtin_amdgcn_make_buffer_rsrc(ws, 0, WS_END, 0x00020000);
  const int n = lane & 15, g = lane >> 4;
  const int cg = kvh * 16 + sp;

  float mc = 0.f, l = 0.f;
  f32x4 o0 = {0.f, 0.f, 0.f, 0.f}, o1 = {0.f, 0.f, 0.f, 0.f};

  if (wave < NSW) {
    const u64 t_start = __builtin_amdgcn_s_memrealtime();
    const size_t kvbase = ((size_t)(b * HKV + kvh) * SK + (size_t)sp * KEYS) * DH;
    const gptr kp = gk + kvbase + (size_t)(n >> 2) * DH + 8 * (4 * g + (n & 3));
    const gptr vp = gv + kvbase + 8 * lane;
    const int rot = ((bid * 5 + (bid >> 3) * 11) & 15) * 8;
    u32x4 kr[W][4], vr[W][4];
#pragma unroll
    for (int t = 0; t < W; ++t)
#pragma unroll
      for (int i = 0; i < 4; ++i) { kr[t][i] = u32x4{0u, 0u, 0u, 0u}; vr[t][i] = u32x4{0u, 0u, 0u, 0u}; }
#define STR2(x) #x
#define STR(x) STR2(x)
#define LDKV(dst, ptr, off, n) asm volatile("s_waitcnt vmcnt(" STR(n) ")\n\tglobal_load_dwordx4 %0, %1, off offset:" STR(off) " nt" : "+v"(dst) : "v"(ptr) : "memory")
#define ISSUE(slot, kq_, vq_, n) do { \
      LDKV(kr[slot][0], kq_, 0, n); LDKV(kr[slot][1], kq_, 1024, n); LDKV(kr[slot][2], kq_, 2048, n); LDKV(kr[slot][3], kq_, 3072, n); \
      LDKV(vr[slot][0], vq_, 0, n); LDKV(vr[slot][1], vq_, 1024, n); LDKV(vr[slot][2], vq_, 2048, n); LDKV(vr[slot][3], vq_, 3072, n); \
      __builtin_amdgcn_sched_barrier(0); } while (0)
    __builtin_amdgcn_s_barrier();
    __builtin_amdgcn_sched_barrier(0);
#pragma unroll
    for (int t = 0; t < W; ++t) {
      const size_t o = (size_t)((t * NSW + wave + rot) & (NTILE - 1)) * (16 * DH);
      const gptr kq = kp + o;
      const gptr vq = vp + o;
      ISSUE(t, kq, vq, INFL_PRO);
    }
    unsigned nxt = W * NSW + wave;
    while (LDS_LD(L_QRDY) == 0u) __builtin_amdgcn_s_sleep(1);
    asm volatile("" ::: "memory");
    const s16x8 qf = *reinterpret_cast<const s16x8*>(lds + L_QF + ((n >> 2) * 16 + 4 * g + (n & 3)) * 16);
    const unsigned jm = n & 3;
    const unsigned mask0 = jm == 0 ? 0x0000ffffu : (jm == 1 ? 0xffff0000u : 0u);
    const unsigned mask1 = jm == 2 ? 0x0000ffffu : (jm == 3 ? 0xffff0000u : 0u);
    const bool sel1 = (lane & 1) != 0, sel2 = (lane & 2) != 0;
    float m = -INFINITY;

#define TILE(slot) do { \
      asm volatile("" : "+v"(kr[slot][0]), "+v"(kr[slot][1]), "+v"(kr[slot][2]), "+v"(kr[slot][3]), \
                        "+v"(vr[slot][0]), "+v"(vr[slot][1]), "+v"(vr[slot][2]), "+v"(vr[slot][3])); \
      float s_[4]; \
      _Pragma("unroll") for (int i_ = 0; i_ < 4; ++i_) { \
        const f32x4 d_ = __builtin_amdgcn_mfma_f32_16x16x32_bf16(__builtin_bit_cast(s16x8, kr[slot][i_]), qf, f32x4{0.f, 0.f, 0.f, 0.f}, 0, 0, 0); \
        const float t0_ = sel1 ? d_[1] : d_[0], t1_ = sel1 ? d_[3] : d_[2]; \
        float u_ = sel2 ? t1_ : t0_; u_ += dpp<XOR1>(u_); u_ += dpp<XOR2>(u_); s_[i_] = u_; } \
      const float mx_ = fmaxf(fmaxf(s_[0], s_[1]), fmaxf(s_[2], s_[3])); \
      if (__builtin_amdgcn_ballot_w64(mx_ > m) != 0) { \
        const float mn_ = fmaxf(m, max_x16_x32(mx_)); \
        const float mcn_ = mn_ * C2; \
        const float al_ = (m == -INFINITY) ? 0.f : __builtin_amdgcn_exp2f(mc - mcn_); \
        l *= al_; m = mn_; mc = mcn_; \
        const float a0_ = asf(__builtin_amdgcn_readlane((int)asu(al_), 0)); \
        const float a1_ = asf(__builtin_amdgcn_readlane((int)asu(al_), 4)); \
        const float a2_ = asf(__builtin_amdgcn_readlane((int)asu(al_), 8)); \
        const float a3_ = asf(__builtin_amdgcn_readlane((int)asu(al_), 12)); \
        const float ao_ = g == 0 ? a0_ : (g == 1 ? a1_ : (g == 2 ? a2_ : a3_)); \
        o0 *= ao_; o1 *= ao_; } \
      _Pragma("unroll") for (int i_ = 0; i_ < 4; ++i_) { \
        const float p_ = __builtin_amdgcn_exp2f(fmaf(s_[i_], C2, -mc)); \
        l += p_; \
        const unsigned pb_ = cvtpk(p_, p_); \
        const u32x2 a_ = {pb_ & mask0, pb_ & mask1}; \
        const u32x2 va_ = {vr[slot][i_][0], vr[slot][i_][1]}, vb_ = {vr[slot][i_][2], vr[slot][i_][3]}; \
        o0 = __builtin_amdgcn_mfma_f32_16x16x16bf16_1k(__builtin_bit_cast(s16x4, a_), __builtin_bit_cast(s16x4, va_), o0, 0, 0, 0); \
        o1 = __builtin_amdgcn_mfma_f32_16x16x16bf16_1k(__builtin_bit_cast(s16x4, a_), __builtin_bit_cast(s16x4, vb_), o1, 0, 0, 0); } \
      asm volatile("" : "+v"(o0), "+v"(o1)); \
    } while (0)

    int left = W;
#pragma unroll 1
    for (;;) {
#pragma unroll
      for (int s = 0; s < W; ++s) {
        TILE(s);
        __builtin_amdgcn_sched_barrier(0);
        const unsigned cur = __builtin_amdgcn_readfirstlane(nxt);
        if (cur < (unsigned)NTILE) {
          nxt = cur + NSW;
          if (cur == (unsigned)(NTILE - 40) && lane == 0) LDS_ST(L_PROG, 1u);
          const size_t o = (size_t)((cur + rot) & (NTILE - 1)) * (16 * DH);
          const gptr kq = kp + o;
          const gptr vq = vp + o;
          const int lagt = (int)(unsigned)(__builtin_amdgcn_s_memrealtime() - t_start) - (CTRL_T0 + (int)(cur >> 3) * CTRL_TR);
          const int mode = __builtin_amdgcn_readfirstlane(lagt > CTRL_BAND ? 3 : (lagt > -CTRL_BAND ? 2 : (lagt > -CTRL_BAND2 ? 1 : 0)));
#define LDKV2(dst, ptr, off) asm volatile("s_cmp_ge_u32 %2, 3\n\ts_cbranch_scc1 3f\n\ts_cmp_eq_u32 %2, 2\n\ts_cbranch_scc1 2f\n\ts_cmp_eq_u32 %2, 1\n\ts_cbranch_scc1 1f\n\ts_waitcnt vmcnt(" STR(INFL_LO2) ")\n1:\n\ts_waitcnt vmcnt(" STR(INFL_LO) ")\n2:\n\ts_waitcnt vmcnt(" STR(INFL_MAIN) ")\n3:\n\ts_waitcnt vmcnt(" STR(INFL_HI) ")\n\tglobal_load_dwordx4 %0, %1, off offset:" STR(off) " nt" : "+v"(dst) : "v"(ptr), "s"(mode) : "memory", "scc")
          LDKV2(kr[s][0], kq, 0); LDKV2(kr[s][1], kq, 1024); LDKV2(kr[s][2], kq, 2048); LDKV2(kr[s][3], kq, 3072);
          LDKV2(vr[s][0], vq, 0); LDKV2(vr[s][1], vq, 1024); LDKV2(vr[s][2], vq, 2048); LDKV2(vr[s][3], vq, 3072);
          __builtin_amdgcn_sched_barrier(0);
        } else {
          if (--left == 0) goto stream_done;
          if (left == 1) asm volatile("s_waitcnt vmcnt(0)" ::: "memory");
          __builtin_amdgcn_sched_barrier(0);
        }
      }
    }
  stream_done:
    ;
#undef TILE
#undef ISSUE
#undef LDKV
#undef LDKV2
  } else {
    __builtin_amdgcn_s_setprio(3);
    const int qw = wave - NSW;
    const bool isq = r < 8, isf1 = r >= 16 && r < 20 && kvh * 4 + r - 16 < 28, slab = isq || isf1;
    u32x4 wv[16], xa = {0u, 0u, 0u, 0u}, xb = xa;
    unsigned short xh[4] = {0, 0, 0, 0};
    if (slab) {
      const int ld = isq ? 4096 : DFF;
      const gptr wp = (isq ? gwq + 64 * (8 * kvh + r) : gw1 + 64 * (4 * kvh + r - 16)) + (size_t)(128 * qw + (lane >> 3)) * ld + 8 * (lane & 7);
      xa = ld16(gx + 8 * lane);
      xb = ld16(gx + DM + 8 * lane);
#pragma unroll
      for (int i = 0; i < 2; ++i) { xh[2 * i] = gx[qw * 128 + 64 * i + lane]; xh[2 * i + 1] = gx[DM + qw * 128 + 64 * i + lane]; }
#pragma unroll
      for (int i = 0; i < 16; ++i) wv[i] = ldnt(wp + (size_t)i * 8 * ld);
    }
    u64 ep = 0; unsigned xo = 0;
    if (qw == 0) {
      ep = ld_g(ws, WS_E);
      xo = *(const GAS unsigned*)(gx + b * DM + 4 * cg + 2 * (lane & 1));
      if (lane == 0) { LDS_ST(L_QCNT, 0u); LDS_ST(L_QRDY, 0u); LDS_ST(L_PROG, 0u); LDS_ST(L_TRDY, 0u); }
    }
    __builtin_amdgcn_sched_barrier(0);
    BARRIER();
    __builtin_amdgcn_sched_barrier(0);
    ep += 1ull;
    if (slab) {
      float m0, s0, m1, s1;
      ln_stats(xa, m0, s0); ln_stats(xb, m1, s1);
#pragma unroll
      for (int i = 0; i < 2; ++i)
        *reinterpret_cast<f32x2*>(lds + L_XN + (qw * 128 + 64 * i + lane) * 8) = f32x2{ln_apply(bf_lo(xh[2 * i]), m0, s0), ln_apply(bf_lo(xh[2 * i + 1]), m1, s1)};
      asm volatile("s_waitcnt lgkmcnt(0)" ::: "memory");
      f32x2 a0[4], a1[4];
#pragma unroll
      for (int c = 0; c < 4; ++c) { a0[c] = f32x2{0.f, 0.f}; a1[c] = f32x2{0.f, 0.f}; }
#pragma unroll
      for (int i = 0; i < 16; ++i) {
        const f32x2 xn = *reinterpret_cast<const f32x2*>(lds + L_XN + (qw * 128 + 8 * i + (lane >> 3)) * 8);
#pragma unroll
        for (int c = 0; c < 4; ++c) { const f32x2 w2 = {bf_lo(wv[i][c]), bf_hi(wv[i][c])}; a0[c] += xn[0] * w2; a1[c] += xn[1] * w2; }
      }
      float t[8];
#pragma unroll
      for (int c = 0; c < 4; ++c) { t[2 * c] = red32(a0[c][0], a1[c][0]); t[2 * c + 1] = red32(a0[c][1], a1[c][1]); }
#pragma unroll
      for (int c = 0; c < 4; ++c) { t[c] = red16(t[c], t[c + 4]); t[c] += dpp<ROR8>(t[c]); }
      if ((lane & 8) == 0)
        *reinterpret_cast<f32x4*>(lds + L_Q + qw * 512 + (lane >> 5) * 256 + (8 * (lane & 7) + 4 * ((lane >> 4) & 1)) * 4) = f32x4{t[0], t[1], t[2], t[3]};
      asm volatile("s_waitcnt lgkmcnt(0)" ::: "memory");
      if (lane == 0) __hip_atomic_fetch_add((lds_u32*)(lds + L_QCNT), 1u, __ATOMIC_RELAXED, __HIP_MEMORY_SCOPE_WORKGROUP);
    }
    if (qw == 0) {
      const unsigned qhi = (unsigned)(ep >> 16), qlo = ((unsigned)ep) << 16;
      if (lane == 0) { LDS_ST(L_TAG, ((unsigned)ep) << 24); LDS_ST(L_TAG + 4, (unsigned)(ep >> 8)); LDS_ST(L_TAG + 8, qlo); LDS_ST(L_TAG + 12, qhi); LDS_ST(L_TAG + 16, (unsigned)ep); LDS_ST(L_TAG + 20, (unsigned)(ep >> 32)); }
      asm volatile("s_waitcnt lgkmcnt(0)" ::: "memory");
      if (lane == 0) LDS_ST(L_TRDY, 1u);
      if (slab) {
        while (LDS_LD(L_QCNT) != (unsigned)NQW) { }
        asm volatile("" ::: "memory");
        f32x2 qs = {0.f, 0.f};
#pragma unroll
        for (int w = 0; w < NQW; ++w) qs += *reinterpret_cast<const f32x2*>(lds + L_Q + w * 512 + lane * 8);
        unsigned pk = cvtpk(qs[0], qs[1]);
        if (!isq) pk = cvtpk(fmaxf(bf_lo(pk), 0.f), fmaxf(bf_hi(pk), 0.f));
        const unsigned dst = isq ? WS_Q + (((lane >> 5) * 8 + kvh) * 512 + r * 64 + 2 * (lane & 31)) * 8
                                 : WS_HX + ((lane >> 5) * 2048 + 64 * (4 * kvh + r - 16) + 2 * (lane & 31)) * 8;
        ST16((u32x4{qlo | (pk & 0xffffu), qhi, qlo | (pk >> 16), qhi}), dst);
      }
      {
        const unsigned qb = WS_Q + (b * 8 + kvh) * 4096 + lane * 64;
        const u64 t0 = __builtin_amdgcn_s_memrealtime();
        if (!slab) while (__builtin_amdgcn_s_memrealtime() - t0 < (u64)QDELAY) __builtin_amdgcn_s_sleep(8);
        u32x4 qv[4];
        for (;;) {
          asm volatile("" ::: "memory");
#pragma unroll
          for (int i = 0; i < 4; ++i) qv[i] = LD16(qb + i * 16);
          unsigned bad = 0;
#pragma unroll
          for (int i = 0; i < 4; ++i) bad |= BAD16(qv[i]);
          if (__builtin_amdgcn_ballot_w64(bad != 0) == 0 || timed_out(t0)) break;
          __builtin_amdgcn_s_sleep(4);
        }
        u32x4 f;
#pragma unroll
        for (int i = 0; i < 4; ++i) f[i] = (qv[i][0] & 0xffffu) | (qv[i][2] << 16);
        *reinterpret_cast<u32x4*>(lds + L_QF + lane * 16) = f;
        asm volatile("s_waitcnt lgkmcnt(0)" ::: "memory");
        if (lane == 0) { LDS_ST(L_QRDY, 1u); st_g(ws, WS_FLAG + bid * 8, ep); }
      }
    } else {
      {
        const gptr wp = gwo + (size_t)(kvh * 512 + (sp >> 2) * 128 + g) * DM + (sp & 3) * 128 + 8 * n;
#pragma unroll
        for (int i = 0; i < 11; ++i) { const int q4 = min((qw - 1) * 11 + i, 31); wv[i] = ldnt(wp + (size_t)q4 * 4 * DM); }
#pragma unroll
        for (int i = 0; i < 11; ++i) { const int q4 = min((qw - 1) * 11 + i, 31); *reinterpret_cast<u32x4*>(lds + L_WO + (q4 * 4 + g) * 256 + ((n ^ (2 * (q4 & 7))) * 16)) = wv[i]; }
        asm volatile("s_waitcnt lgkmcnt(0)" ::: "memory");
      }
      if (qw != 3) return;
      while (LDS_LD(L_TRDY) == 0u) { }
      asm volatile("" ::: "memory");
      const unsigned tlo = LDS_LD(L_TAG), thi = LDS_LD(L_TAG + 4), qlo = LDS_LD(L_TAG + 8), qhi = LDS_LD(L_TAG + 12);
      if (r >= 8 && r < 16) {
        u32x4 w2v[28];
        const gptr wp = gw2 + (size_t)(224 * kvh + (lane >> 3)) * DM + 64 * (r - 8) + 8 * (lane & 7);
#pragma unroll
        for (int i = 0; i < 28; ++i) w2v[i] = ldnt(wp + (size_t)i * 8 * DM);
        const unsigned hb = WS_HX + (224 * kvh + 2 * lane) * 8;
        const u64 t0 = __builtin_amdgcn_s_memrealtime();
        u32x4 hv[4];
        for (;;) {
          asm volatile("" ::: "memory");
          hv[0] = LD16(hb); hv[1] = LD16(hb + 1024); hv[2] = LD16(hb + 16384); hv[3] = LD16(hb + 16384 + 1024);
          const unsigned bad = BAD16(hv[0]) | BAD16(hv[2]) | (lane < 48 ? BAD16(hv[1]) | BAD16(hv[3]) : 0u);
          if (__builtin_amdgcn_ballot_w64(bad != 0) == 0 || timed_out(t0)) break;
          __builtin_amdgcn_s_sleep(64);
        }
        *reinterpret_cast<f32x4*>(lds + L_XN + lane * 16) = f32x4{bf_lo(hv[0][0]), bf_lo(hv[2][0]), bf_lo(hv[0][2]), bf_lo(hv[2][2])};
        *reinterpret_cast<f32x4*>(lds + L_XN + 1024 + lane * 16) = f32x4{bf_lo(hv[1][0]), bf_lo(hv[3][0]), bf_lo(hv[1][2]), bf_lo(hv[3][2])};
        asm volatile("s_waitcnt lgkmcnt(0)" ::: "memory");
        f32x2 a0[4], a1[4];
#pragma unroll
        for (int c = 0; c < 4; ++c) { a0[c] = f32x2{0.f, 0.f}; a1[c] = f32x2{0.f, 0.f}; }
#pragma unroll
        for (int i = 0; i < 28; ++i) {
          const f32x2 hn = *reinterpret_cast<const f32x2*>(lds + L_XN + (8 * i + (lane >> 3)) * 8);
#pragma unroll
          for (int c = 0; c < 4; ++c) { const f32x2 w2 = {bf_lo(w2v[i][c]), bf_hi(w2v[i][c])}; a0[c] += hn[0] * w2; a1[c] += hn[1] * w2; }
        }
        float t[8];
#pragma unroll
        for (int c = 0; c < 4; ++c) { t[2 * c] = red32(a0[c][0], a1[c][0]); t[2 * c + 1] = red32(a0[c][1], a1[c][1]); }
#pragma unroll
        for (int c = 0; c < 4; ++c) { t[c] = red16(t[c], t[c + 4]); t[c] += dpp<ROR8>(t[c]); }
        const float y0 = (lane & 8) ? t[2] : t[0], y1 = (lane & 8) ? t[3] : t[1];
        ST16((u32x4{f24(y0, tlo), thi, f24(y1, tlo), thi}),
             WS_YX + ((kvh * 8 + r - 8) * 128 + (lane >> 5) * 64 + 8 * (lane & 7) + 4 * ((lane >> 4) & 1) + 2 * ((lane >> 3) & 1)) * 8);
      }
      while (LDS_LD(L_PROG) == 0u) __builtin_amdgcn_s_sleep(32);
      {
        const int jh_ = sp >> 2, jc_ = sp & 3;
        const unsigned gb_ = WS_H1 + (unsigned)(kvh + 128 * b) * OBX;
        unsigned acc_ = 0;
        acc_ ^= __builtin_amdgcn_raw_buffer_load_b32(rsrc, WS_H1 + bid * OBX + (lane < 36 ? lane : 0) * 128, 0, 16);
#pragma unroll
        for (int i = 0; i < 3; ++i) { const int e = min(64 * i + lane, 143); acc_ ^= __builtin_amdgcn_raw_buffer_load_b32(rsrc, gb_ + (e / 9) * 8 * OBX + jh_ * 1152 + (e % 9) * 128, 0, 16); }
        acc_ ^= __builtin_amdgcn_raw_buffer_load_b32(rsrc, lane < 32 ? WS_H2 + ((b * 32 + (cg >> 2)) * 32 + lane) * 128
                                                              : WS_H2 + ((b * 32 + jc_ * 8 + (lane & 7)) * 32 + kvh * 4 + jh_) * 128, 0, 16);
        acc_ ^= *(const GAS unsigned*)(p.out + b * DM + 4 * cg);
        asm volatile("" :: "v"(acc_));
      }
      if (bid == 0) {
        const unsigned elo = LDS_LD(L_TAG + 16), ehi = LDS_LD(L_TAG + 20);
        const u64 t0 = __builtin_amdgcn_s_memrealtime();
        for (;;) {
          asm volatile("" ::: "memory");
          const u32x4 f0 = LD16(WS_FLAG + lane * 16), f1 = LD16(WS_FLAG + 1024 + lane * 16);
          const unsigned bad = (f0[0] ^ elo) | (f0[1] ^ ehi) | (f0[2] ^ elo) | (f0[3] ^ ehi) | (f1[0] ^ elo) | (f1[1] ^ ehi) | (f1[2] ^ elo) | (f1[3] ^ ehi);
          if (__builtin_amdgcn_ballot_w64(bad != 0) == 0 || timed_out(t0)) break;
          __builtin_amdgcn_s_sleep(32);
        }
        if (lane == 0) st_g(ws, WS_E, ((u64)ehi << 32) | elo);
      }
      {
        const unsigned yb = WS_YX + ((((lane >> 1) & 7) * 8 + (cg >> 4)) * 128 + b * 64 + (cg & 15) * 4 + 2 * (lane & 1)) * 8;
        const u64 t0 = __builtin_amdgcn_s_memrealtime();
        u32x4 yv;
        for (;;) {
          asm volatile("" ::: "memory");
          yv = LD16(yb);
          if (__builtin_amdgcn_ballot_w64(BAD24(yv) != 0) == 0 || timed_out(t0)) break;
          __builtin_amdgcn_s_sleep(32);
        }
        const float ya = rbf(row_sum8_odd_even(uf24(yv[0]))), yc = rbf(row_sum8_odd_even(uf24(yv[2])));
        if (lane < 2) *reinterpret_cast<f32x2*>(lds + L_GATE + lane * 8) = f32x2{rbf(ya / (1.f + __expf(-ya))), rbf(yc / (1.f + __expf(-yc)))};
        asm volatile("s_waitcnt lgkmcnt(0)" ::: "memory");
      }
      return;
    }
    const unsigned thi = (unsigned)(ep >> 8), tlo = ((unsigned)ep) << 24;
    BARRIER();
    __builtin_amdgcn_s_setprio(0);
    BARRIER();
    __builtin_amdgcn_s_sleep(4);
    {
      const f32x2 gate = *reinterpret_cast<const f32x2*>(lds + L_GATE + (lane & 1) * 8);
      const unsigned ib = WS_H2 + (((b * 32 + (cg >> 2)) * 32 + (lane >> 1)) * 16 + (cg & 3) * 4 + 2 * (lane & 1)) * 8;
      const u64 t0 = __builtin_amdgcn_s_memrealtime();
      u32x4 sv = {0u, ~thi, 0u, 0u};
      for (;;) {
        asm volatile("" ::: "memory");
        if (BAD24(sv) != 0) sv = LD16(ib);
        if (__builtin_amdgcn_ballot_w64(BAD24(sv) != 0) == 0 || timed_out(t0)) break;
      }
      float s0 = row_sum8_odd_even(uf24(sv[0])); s0 = red16(s0, s0); s0 = red32(s0, s0);
      float s1 = row_sum8_odd_even(uf24(sv[2])); s1 = red16(s1, s1); s1 = red32(s1, s1);
      const float r0 = bf_lo(xo) + rbf(rbf(s0) * gate[0]), r1 = bf_hi(xo) + rbf(rbf(s1) * gate[1]);
      if (lane < 2) *(GAS unsigned*)(p.out + b * DM + 4 * cg + 2 * lane) = cvtpk(r0, r1);
    }
    return;
  }

  {
    l = red16(l, l); l = red32(l, l);
    *reinterpret_cast<f32x4*>(lds + L_O + wave * 2048 + g * 512 + n * 32) = o0;
    *reinterpret_cast<f32x4*>(lds + L_O + wave * 2048 + g * 512 + n * 32 + 16) = o1;
    if ((lane & 0x33) == 0) {
      *reinterpret_cast<float*>(lds + L_M + wave * 16 + (n >> 2) * 4) = mc;
      *reinterpret_cast<float*>(lds + L_L + wave * 16 + (n >> 2) * 4) = l;
    }
    const unsigned tlo = LDS_LD(L_TAG), thi = LDS_LD(L_TAG + 4);
    BARRIER();
    if (wave < 4) {
      float mw[NSW], M = -INFINITY;
#pragma unroll
      for (int w = 0; w < NSW; ++w) { mw[w] = *reinterpret_cast<const float*>(lds + L_M + w * 16 + wave * 4); M = fmaxf(M, mw[w]); }
      M = asf(asu(M) & 0xffffff00u);
      float Oa = 0.f, Ob = 0.f, L = 0.f;
#pragma unroll
      for (int w = 0; w < NSW; ++w) {
        const float wt = __builtin_amdgcn_exp2f(mw[w] - M);
        const f32x2 ov = *reinterpret_cast<const f32x2*>(lds + L_O + w * 2048 + wave * 512 + lane * 8);
        Oa = fmaf(wt, ov[0], Oa); Ob = fmaf(wt, ov[1], Ob);
        L = fmaf(wt, *reinterpret_cast<const float*>(lds + L_L + w * 16 + wave * 4), L);
      }
      ST16((u32x4{f24(Oa, tlo), thi, f24(Ob, tlo), thi}), WS_H1 + bid * OBX + wave * 1152 + lane * 16);
      if (lane == 0) ST16((u32x4{f24(M, tlo), thi, f24(L, tlo), thi}), WS_H1 + bid * OBX + wave * 1152 + 1024);
    }
    const int pw = wave & 3;
    f32x2 wf[2][4][4];
    if (wave >= 4) {
#pragma unroll
      for (int cb = 0; cb < 2; ++cb)
#pragma unroll
        for (int i = 0; i < 4; ++i) {
          const u32x4 wv = *reinterpret_cast<const u32x4*>(lds + L_WO + (4 * (lane >> 1) + i) * 256 + (((4 * pw + 2 * cb + (lane & 1)) ^ (lane & 14)) * 16));
#pragma unroll
          for (int c = 0; c < 4; ++c) { wf[cb][i][c] = f32x2{bf_lo(wv[c]), bf_hi(wv[c])}; asm volatile("" : "+v"(wf[cb][i][c])); }
        }
    }
    const int jh = sp >> 2, jc = sp & 3;
    {
      const unsigned gb = WS_H1 + (unsigned)(kvh + 128 * b) * OBX;
      const unsigned oa_off = gb + (2 * wave) * 8 * OBX + jh * 1152 + lane * 16, ml_off = gb + (2 * wave + (lane & 1)) * 8 * OBX + jh * 1152 + 1024;
      const u64 t0 = __builtin_amdgcn_s_memrealtime();
      u32x4 va = {0u, ~thi, 0u, 0u}, vb = va, vm = va;
      for (;;) {
        asm volatile("" ::: "memory");
        if (BAD24(va) != 0) va = LD16(oa_off);
        if (BAD24(vb) != 0) vb = LD16(oa_off + 8 * OBX);
        if (BAD24(vm) != 0) vm = LD16(ml_off);
        if (__builtin_amdgcn_ballot_w64((BAD24(va) | BAD24(vb) | BAD24(vm)) != 0) == 0 || timed_out(t0)) break;
      }
      const float Ms = uf24(vm[0]), M = fmaxf(Ms, dpp<XOR1>(Ms));
      const float wt = __builtin_amdgcn_exp2f(Ms - M), wo_ = dpp<XOR1>(wt);
      const float wl = wt * uf24(vm[2]), L = wl + dpp<XOR1>(wl);
      const float wa = (lane & 1) ? wo_ : wt, wb = (lane & 1) ? wt : wo_;
      *reinterpret_cast<f32x2*>(lds + L_OP + wave * 512 + lane * 8) = f32x2{fmaf(wa, uf24(va[0]), wb * uf24(vb[0])), fmaf(wa, uf24(va[2]), wb * uf24(vb[2]))};
      if (lane == 0) *reinterpret_cast<f32x2*>(lds + L_ML2 + wave * 8) = f32x2{M, L};
    }
    BARRIER();
    if (wave >= 4) {
      f32x4 ml[NSW / 2], op[NSW];
#pragma unroll
      for (int w = 0; w < NSW / 2; ++w) ml[w] = *reinterpret_cast<const f32x4*>(lds + L_ML2 + w * 16);
#pragma unroll
      for (int w = 0; w < NSW; ++w) op[w] = *reinterpret_cast<const f32x4*>(lds + L_OP + w * 512 + (lane >> 1) * 16);
      __builtin_amdgcn_sched_barrier(0);
      float M = -INFINITY, L = 0.f, cw[NSW];
#pragma unroll
      for (int w = 0; w < NSW / 2; ++w) M = fmaxf(M, fmaxf(ml[w][0], ml[w][2]));
#pragma unroll
      for (int w = 0; w < NSW / 2; ++w) {
        cw[2 * w] = __builtin_amdgcn_exp2f(ml[w][0] - M); cw[2 * w + 1] = __builtin_amdgcn_exp2f(ml[w][2] - M);
        L = fmaf(cw[2 * w], ml[w][1], L); L = fmaf(cw[2 * w + 1], ml[w][3], L);
      }
      f32x4 ov = cw[0] * op[0];
#pragma unroll
      for (int w = 1; w < NSW; ++w) ov += cw[w] * op[w];
      float inv = __builtin_amdgcn_rcpf(L);
      inv = fmaf(fmaf(-L, inv, 1.0f), inv, inv);
      float o[4];
#pragma unroll
      for (int i = 0; i < 4; ++i) o[i] = rbf(ov[i] * inv);
      float e[2][2];
#pragma unroll
      for (int cb = 0; cb < 2; ++cb) {
        f32x2 ac[4];
#pragma unroll
        for (int i = 0; i < 4; ++i)
#pragma unroll
          for (int c = 0; c < 4; ++c) ac[c] = i == 0 ? o[0] * wf[cb][0][c] : ac[c] + o[i] * wf[cb][i][c];
        float acc[4];
        acc[0] = red32(ac[0][0], ac[2][0]); acc[1] = red32(ac[0][1], ac[2][1]);
        acc[2] = red32(ac[1][0], ac[3][0]); acc[3] = red32(ac[1][1], ac[3][1]);
#pragma unroll
        for (int c = 0; c < 2; ++c) e[cb][c] = row_sum8_odd_even(red16(acc[c], acc[c + 2]));
      }
      const float e0 = (lane & 2) ? e[1][0] : e[0][0], e1 = (lane & 2) ? e[1][1] : e[0][1];
      if ((lane & 12) == 0)
        ST16((u32x4{f24(e0, tlo), thi, f24(e1, tlo), thi}),
             WS_H2 + (((b * 32 + jc * 8 + 2 * pw + ((lane >> 1) & 1)) * 32 + kvh * 4 + jh) * 16 + 8 * (lane & 1) + 4 * (lane >> 5) + 2 * ((lane >> 4) & 1)) * 8);
    }
  }
}

}

extern "C" void kernel_launch(void* const* d_in, const int* in_sizes, int n_in, void* d_out, int out_size, void* d_ws,
                              size_t ws_size, hipStream_t stream) {
  Params p;
  p.x = (const unsigned short*)d_in[0]; p.k = (const unsigned short*)d_in[1]; p.v = (const unsigned short*)d_in[2];
  p.wq = (const unsigned short*)d_in[3]; p.wo = (const unsigned short*)d_in[4];
  p.w1 = (const unsigned short*)d_in[6]; p.w2 = (const unsigned short*)d_in[7];
  p.out = (unsigned short*)d_out; p.ws = (unsigned char*)d_ws;
  hipLaunchKernelGGL(fused_decode, dim3(256), dim3(NTHR), 0, stream, p);
}
```

```cpp
#include <hip/hip_runtime.h>
#include <hip/hip_bf16.h>
#include <cstdint>

#define COMPACT 1
#define PRETOUCH 16
#define ROTMODE 0
namespace {

typedef unsigned u32x4 __attribute__((ext_vector_type(4)));
typedef unsigned u32x2 __attribute__((ext_vector_type(2)));
typedef float f32x4 __attribute__((ext_vector_type(4)));
typedef float f32x2 __attribute__((ext_vector_type(2)));
typedef __bf16 bf16x2 __attribute__((ext_vector_type(2)));
typedef short s16x8 __attribute__((ext_vector_type(8)));
typedef short s16x4 __attribute__((ext_vector_type(4)));
typedef unsigned long long u64;
#define GAS __attribute__((address_space(1)))
typedef GAS u64 gu64;
typedef const GAS unsigned short* gptr;

constexpr int SK = 32768, DH = 128, DM = 512, DFF = 1792, HKV = 8;
constexpr int NSPLIT = 16, KEYS = SK / NSPLIT;
constexpr int NSW = 8;
constexpr int NQW = 4;
#define INFL_PRO 3
#define INFL_MAIN 4
#define END_TILES 4
#define H0SLEEP 4
#define QDELAY 600
#define QSLEEP 4
constexpr int NTHR = (NSW + NQW) * 64;
constexpr int NTILE = KEYS / 16;
constexpr int W = 4;

constexpr unsigned WS_E = 0;
constexpr unsigned WS_FLAG = 0x1000;
constexpr unsigned WS_Q = 0x2000;
constexpr unsigned WS_HX = 0x12000;
constexpr unsigned WS_YX = 0x20000;
constexpr unsigned OBX = 4608;
constexpr unsigned WS_H2 = 0x30000;
constexpr unsigned WS_H1 = 0x70000;
constexpr unsigned WS_END = WS_H1 + 256 * OBX;

constexpr float C2 = 0.08838834764831845f * 1.4426950408889634f;
constexpr u64 SPIN_TICKS = 4000000ull;

constexpr int L_O = 0;
constexpr int L_M = L_O + NSW * 2048;
constexpr int L_L = L_M + NSW * 16;
constexpr int L_Q = L_L + NSW * 16;
constexpr int L_XN = L_Q + 4 * 512;
constexpr int L_QF = L_XN + 4096;
constexpr int L_OP = L_QF + 1024;
constexpr int L_ML2 = L_OP + NSW * 512;
constexpr int L_WO = L_ML2 + NSW * 8;
constexpr int L_TAG = L_WO + 128 * 256;
constexpr int L_GATE = L_TAG + 32;
constexpr int L_QCNT = L_GATE + 16;
constexpr int L_QRDY = L_QCNT + 4;
constexpr int L_PROG = L_QCNT + 8;
constexpr int L_TICK = L_QCNT + 12;
constexpr int L_TRDY = L_QCNT + 16;
constexpr int L_END = L_QCNT + 32;

__device__ __forceinline__ float asf(unsigned u) { return __builtin_bit_cast(float, u); }
__device__ __forceinline__ unsigned asu(float f) { return __builtin_bit_cast(unsigned, f); }
__device__ __forceinline__ float bf_lo(unsigned u) { return asf(u << 16); }
__device__ __forceinline__ float bf_hi(unsigned u) { return asf(u & 0xffff0000u); }
__device__ __forceinline__ unsigned cvtpk(float lo, float hi) {
  const f32x2 v = {lo, hi};
  return __builtin_bit_cast(unsigned, __builtin_convertvector(v, bf16x2));
}
__device__ __forceinline__ float rbf(float x) { return bf_lo(cvtpk(x, x)); }
template <int CTRL> __device__ __forceinline__ float dpp(float x) {
  return asf(__builtin_amdgcn_mov_dpp((int)asu(x), CTRL, 0xf, 0xf, true));
}
constexpr int XOR1 = 0xB1, XOR2 = 0x4E, HMIRR = 0x141, MIRR = 0x140, ROR4 = 0x124, ROR8 = 0x128;
__device__ __forceinline__ float red32(float a, float b) {
  auto r = __builtin_amdgcn_permlane32_swap(asu(a), asu(b), false, false);
  return asf((unsigned)r[0]) + asf((unsigned)r[1]);
}
__device__ __forceinline__ float red16(float a, float b) {
  auto r = __builtin_amdgcn_permlane16_swap(asu(a), asu(b), false, false);
  return asf((unsigned)r[0]) + asf((unsigned)r[1]);
}
__device__ __forceinline__ float max_x16_x32(float x) {
  auto s = __builtin_amdgcn_permlane16_swap(asu(x), asu(x), false, false);
  x = fmaxf(asf((unsigned)s[0]), asf((unsigned)s[1]));
  auto t = __builtin_amdgcn_permlane32_swap(asu(x), asu(x), false, false);
  return fmaxf(asf((unsigned)t[0]), asf((unsigned)t[1]));
}
__device__ __forceinline__ float row_sum16(float x) {
  x += dpp<XOR1>(x); x += dpp<XOR2>(x); x += dpp<HMIRR>(x); x += dpp<MIRR>(x); return x;
}
__device__ __forceinline__ float row_sum8_odd_even(float t) {
  t += dpp<ROR8>(t); t += dpp<ROR4>(t); t += dpp<XOR2>(t); return t;
}
__device__ __forceinline__ float wave_sum(float x) { x = row_sum16(x); x = red16(x, x); return red32(x, x); }
__device__ __forceinline__ u32x4 ldnt(gptr p) { return __builtin_nontemporal_load((const GAS u32x4*)p); }
__device__ __forceinline__ u32x4 ld16(gptr p) { return *(const GAS u32x4*)p; }
__device__ __forceinline__ void st_g(unsigned char* ws, unsigned off, u64 v) {
  __hip_atomic_store((gu64*)(ws + off), v, __ATOMIC_RELAXED, __HIP_MEMORY_SCOPE_AGENT);
}
__device__ __forceinline__ u64 ld_g(unsigned char* ws, unsigned off) {
  return __hip_atomic_load((gu64*)(ws + off), __ATOMIC_RELAXED, __HIP_MEMORY_SCOPE_AGENT);
}
__device__ __forceinline__ unsigned f24(float x, unsigned tlo) { const unsigned u = asu(x); return ((u + 0x7fu + ((u >> 8) & 1u)) >> 8) | tlo; }
__device__ __forceinline__ float uf24(unsigned lo) { return asf(lo << 8); }
__device__ __forceinline__ u64 gran24(float x, unsigned tlo, unsigned thi) { return ((u64)thi << 32) | f24(x, tlo); }
#define BAD24(v) (((v)[1] ^ thi) | ((v)[3] ^ thi) | (((v)[0] ^ tlo) >> 24) | (((v)[2] ^ tlo) >> 24))
#define BAD16(v) (((v)[1] ^ qhi) | ((v)[3] ^ qhi) | (((v)[0] ^ qlo) >> 16) | (((v)[2] ^ qlo) >> 16))
#define LD16(off) __builtin_amdgcn_raw_buffer_load_b128(rsrc, (off), 0, 16)
#define ST16(v, off) __builtin_amdgcn_raw_buffer_store_b128((v), rsrc, (off), 0, 16)
__device__ __forceinline__ bool timed_out(u64 t0) { return __builtin_amdgcn_s_memrealtime() - t0 > SPIN_TICKS; }
#define STAMP(i) do {} while (0)
#define STAMP_DUMP() do {} while (0)
#define BARRIER() do { asm volatile("s_waitcnt lgkmcnt(0)" ::: "memory"); __builtin_amdgcn_s_barrier(); asm volatile("" ::: "memory"); } while (0)
typedef __attribute__((address_space(3))) unsigned lds_u32;
#define LDS_LD(off) __hip_atomic_load((lds_u32*)(lds + (off)), __ATOMIC_RELAXED, __HIP_MEMORY_SCOPE_WORKGROUP)
#define LDS_ST(off, v) __hip_atomic_store((lds_u32*)(lds + (off)), (v), __ATOMIC_RELAXED, __HIP_MEMORY_SCOPE_WORKGROUP)

__device__ __forceinline__ void ln_stats(u32x4 xv, float& mb, float& sd) {
  float e[8];
#pragma unroll
  for (int i = 0; i < 4; ++i) { e[2 * i] = bf_lo(xv[i]); e[2 * i + 1] = bf_hi(xv[i]); }
  float s = ((e[0] + e[1]) + (e[2] + e[3])) + ((e[4] + e[5]) + (e[6] + e[7]));
  const float mean = wave_sum(s) * (1.0f / DM);
  float q = 0.f;
#pragma unroll
  for (int i = 0; i < 8; ++i) { const float d = e[i] - mean; q = fmaf(d, d, q); }
  const float var = wave_sum(q) * (1.0f / DM);
  mb = rbf(mean);
  sd = rbf(sqrtf(rbf(rbf(var) + 1e-5f)));
}
__device__ __forceinline__ float ln_apply(float x, float mb, float sd) { return rbf(rbf(x - mb) / sd); }

struct Params {
  const unsigned short* x; const unsigned short* k; const unsigned short* v; const unsigned short* wq;
  const unsigned short* wo; const unsigned short* w1; const unsigned short* w2; unsigned short* out; unsigned char* ws;
};

__global__ void __launch_bounds__(NTHR, 1) fused_decode(Params p) {
  __shared__ __attribute__((aligned(16))) unsigned char lds[L_END];
  asm volatile("" : "+s"(p.x), "+s"(p.k), "+s"(p.v), "+s"(p.wq), "+s"(p.wo), "+s"(p.w1), "+s"(p.w2), "+s"(p.out), "+s"(p.ws));
  const gptr gx = (gptr)p.x, gk = (gptr)p.k, gv = (gptr)p.v, gwq = (gptr)p.wq, gwo = (gptr)p.wo, gw1 = (gptr)p.w1, gw2 = (gptr)p.w2;
  const int tid = threadIdx.x, lane = tid & 63;
  const int wave = __builtin_amdgcn_readfirstlane(tid >> 6);
  const int bid = blockIdx.x, kvh = bid & 7, r = bid >> 3, b = r >> 4, sp = r & 15;
  unsigned char* ws = p.ws;
  const auto rsrc = __builtin_amdgcn_make_buffer_rsrc(ws, 0, WS_END, 0x00020000);
  const int n = lane & 15, g = lane >> 4;
  const int cg = kvh * 16 + sp;

  float mc = 0.f, l = 0.f;
  f32x4 o0 = {0.f, 0.f, 0.f, 0.f}, o1 = {0.f, 0.f, 0.f, 0.f};
  int dry = 0, tw = wave;

  if (wave < NSW) {
    const size_t kvbase = ((size_t)(b * HKV + kvh) * SK + (size_t)sp * KEYS) * DH;
#define TOFF(u) ((size_t)(u) * (16 * DH))
    const gptr kp = gk + kvbase + (size_t)(n >> 2) * DH + 8 * (4 * g + (n & 3));
    const gptr vp = gv + kvbase + 8 * lane;
    const int rot = ((bid * 5 + (bid >> 3) * 11) & 15) * 8;
    u32x4 kr[W][4], vr[W][4];
#pragma unroll
    for (int t = 0; t < W; ++t)
#pragma unroll
      for (int i = 0; i < 4; ++i) { kr[t][i] = u32x4{0u, 0u, 0u, 0u}; vr[t][i] = u32x4{0u, 0u, 0u, 0u}; }
#define STR2(x) #x
#define STR(x) STR2(x)
#define LDKV(dst, ptr, off, n) asm volatile("s_waitcnt vmcnt(" STR(n) ")\n\tglobal_load_dwordx4 %0, %1, off offset:" STR(off) " nt" : "+v"(dst) : "v"(ptr) : "memory")
#define ISSUE(slot, kq_, vq_, n) do { \
      LDKV(kr[slot][0], kq_, 0, n); LDKV(kr[slot][1], kq_, 1024, n); LDKV(kr[slot][2], kq_, 2048, n); LDKV(kr[slot][3], kq_, 3072, n); \
      LDKV(vr[slot][0], vq_, 0, n); LDKV(vr[slot][1], vq_, 1024, n); LDKV(vr[slot][2], vq_, 2048, n); LDKV(vr[slot][3], vq_, 3072, n); \
      __builtin_amdgcn_sched_barrier(0); } while (0)
#define TICKET() ({ unsigned t_ = 0; if (lane == 0) t_ = __hip_atomic_fetch_add((lds_u32*)(lds + L_TICK), 1u, __ATOMIC_RELAXED, __HIP_MEMORY_SCOPE_WORKGROUP); t_; })
    STAMP(3);
    __builtin_amdgcn_s_barrier();
    __builtin_amdgcn_sched_barrier(0);
    STAMP(10);
#pragma unroll
    for (int t = 0; t < W; ++t) {
      const size_t o = TOFF((t * NSW + wave + rot) & (NTILE - 1));
      const gptr kq = kp + o;
      const gptr vq = vp + o;
      ISSUE(t, kq, vq, INFL_PRO);
      if (t == 0) STAMP(11);
    }
    STAMP(0);
    unsigned nxt = W * NSW + wave;
    while (LDS_LD(L_QRDY) == 0u) __builtin_amdgcn_s_sleep(1);
    asm volatile("" ::: "memory");
    const s16x8 qf = *reinterpret_cast<const s16x8*>(lds + L_QF + ((n >> 2) * 16 + 4 * g + (n & 3)) * 16);
    const unsigned jm = n & 3;
    const unsigned mask0 = jm == 0 ? 0x0000ffffu : (jm == 1 ? 0xffff0000u : 0u);
    const unsigned mask1 = jm == 2 ? 0x0000ffffu : (jm == 3 ? 0xffff0000u : 0u);
    const bool sel1 = (lane & 1) != 0, sel2 = (lane & 2) != 0;
    float m = -INFINITY;
    STAMP(1);

#define TILE(slot) do { \
      asm volatile("" : "+v"(kr[slot][0]), "+v"(kr[slot][1]), "+v"(kr[slot][2]), "+v"(kr[slot][3]), \
                        "+v"(vr[slot][0]), "+v"(vr[slot][1]), "+v"(vr[slot][2]), "+v"(vr[slot][3])); \
      float s_[4]; \
      _Pragma("unroll") for (int i_ = 0; i_ < 4; ++i_) { \
        const f32x4 d_ = __builtin_amdgcn_mfma_f32_16x16x32_bf16(__builtin_bit_cast(s16x8, kr[slot][i_]), qf, f32x4{0.f, 0.f, 0.f, 0.f}, 0, 0, 0); \
        const float t0_ = sel1 ? d_[1] : d_[0], t1_ = sel1 ? d_[3] : d_[2]; \
        float u_ = sel2 ? t1_ : t0_; u_ += dpp<XOR1>(u_); u_ += dpp<XOR2>(u_); s_[i_] = u_; } \
      const float mx_ = fmaxf(fmaxf(s_[0], s_[1]), fmaxf(s_[2], s_[3])); \
      if (__builtin_amdgcn_ballot_w64(mx_ > m) != 0) { \
        const float mn_ = fmaxf(m, max_x16_x32(mx_)); \
        const float mcn_ = mn_ * C2; \
        const float al_ = (m == -INFINITY) ? 0.f : __builtin_amdgcn_exp2f(mc - mcn_); \
        l *= al_; m = mn_; mc = mcn_; \
        const float a0_ = asf(__builtin_amdgcn_readlane((int)asu(al_), 0)); \
        const float a1_ = asf(__builtin_amdgcn_readlane((int)asu(al_), 4)); \
        const float a2_ = asf(__builtin_amdgcn_readlane((int)asu(al_), 8)); \
        const float a3_ = asf(__builtin_amdgcn_readlane((int)asu(al_), 12)); \
        const float ao_ = g == 0 ? a0_ : (g == 1 ? a1_ : (g == 2 ? a2_ : a3_)); \
        o0 *= ao_; o1 *= ao_; } \
      _Pragma("unroll") for (int i_ = 0; i_ < 4; ++i_) { \
        const float p_ = __builtin_amdgcn_exp2f(fmaf(s_[i_], C2, -mc)); \
        l += p_; \
        const unsigned pb_ = cvtpk(p_, p_); \
        const u32x2 a_ = {pb_ & mask0, pb_ & mask1}; \
        const u32x2 va_ = {vr[slot][i_][0], vr[slot][i_][1]}, vb_ = {vr[slot][i_][2], vr[slot][i_][3]}; \
        o0 = __builtin_amdgcn_mfma_f32_16x16x16bf16_1k(__builtin_bit_cast(s16x4, a_), __builtin_bit_cast(s16x4, va_), o0, 0, 0, 0); \
        o1 = __builtin_amdgcn_mfma_f32_16x16x16bf16_1k(__builtin_bit_cast(s16x4, a_), __builtin_bit_cast(s16x4, vb_), o1, 0, 0, 0); } \
      asm volatile("" : "+v"(o0), "+v"(o1)); \
    } while (0)

    int left = W;
#pragma unroll 1
    for (;;) {
#pragma unroll
      for (int s = 0; s < W; ++s) {
        TILE(s);
        __builtin_amdgcn_sched_barrier(0);
        const unsigned cur = __builtin_amdgcn_readfirstlane(nxt);
        if (cur < (unsigned)NTILE) {
          nxt = cur + NSW;
          if (cur == (unsigned)(NTILE - 40) && lane == 0) LDS_ST(L_PROG, 1u);
          const size_t o = TOFF((cur + rot) & (NTILE - 1));
          const gptr kq = kp + o;
          const gptr vq = vp + o;
          ISSUE(s, kq, vq, INFL_MAIN);
        } else {
          if (--left == 0) goto stream_done;
          if (left == 1) asm volatile("s_waitcnt vmcnt(0)" ::: "memory");
          __builtin_amdgcn_sched_barrier(0);
        }
      }
    }
  stream_done:
    ;
#undef TILE
#undef ISSUE
#undef LDKV
#undef TICKET
  } else {
    __builtin_amdgcn_s_setprio(3);
    const int qw = wave - NSW;
    STAMP(0);
    const bool isq = r < 8, isf1 = r >= 16 && r < 20 && kvh * 4 + r - 16 < 28, slab = isq || isf1;
    u32x4 wv[16];
    if (slab) {
      const int ld = isq ? 4096 : DFF;
      const gptr wp = (isq ? gwq + 64 * (8 * kvh + r) : gw1 + 64 * (4 * kvh + r - 16)) + (size_t)(128 * qw + (lane >> 3)) * ld + 8 * (lane & 7);
#pragma unroll
      for (int i = 0; i < 16; ++i) wv[i] = ldnt(wp + (size_t)i * 8 * ld);
    }
    const u32x4 xa = ld16(gx + 8 * lane);
    const u32x4 xb = ld16(gx + DM + 8 * lane);
    unsigned short xh[4];
#pragma unroll
    for (int i = 0; i < 2; ++i) { xh[2 * i] = gx[qw * 128 + 64 * i + lane]; xh[2 * i + 1] = gx[DM + qw * 128 + 64 * i + lane]; }
    u64 ep = 0; unsigned xo = 0;
    if (qw == 0) {
      ep = ld_g(ws, WS_E);
      xo = *(const GAS unsigned*)(gx + b * DM + 4 * cg + 2 * (lane & 1));
      if (lane == 0) { LDS_ST(L_QCNT, 0u); LDS_ST(L_QRDY, 0u); LDS_ST(L_PROG, 0u); LDS_ST(L_TICK, (unsigned)(W * NSW)); LDS_ST(L_TRDY, 0u); }
    }
    __builtin_amdgcn_sched_barrier(0);
    BARRIER();
    __builtin_amdgcn_sched_barrier(0);
    ep += 1ull;
    if (slab) {
      float m0, s0, m1, s1;
      ln_stats(xa, m0, s0); ln_stats(xb, m1, s1);
#pragma unroll
      for (int i = 0; i < 2; ++i)
        *reinterpret_cast<f32x2*>(lds + L_XN + (qw * 128 + 64 * i + lane) * 8) = f32x2{ln_apply(bf_lo(xh[2 * i]), m0, s0), ln_apply(bf_lo(xh[2 * i + 1]), m1, s1)};
      asm volatile("s_waitcnt lgkmcnt(0)" ::: "memory");
      f32x2 a0[4], a1[4];
#pragma unroll
      for (int c = 0; c < 4; ++c) { a0[c] = f32x2{0.f, 0.f}; a1[c] = f32x2{0.f, 0.f}; }
#pragma unroll
      for (int i = 0; i < 16; ++i) {
        const f32x2 xn = *reinterpret_cast<const f32x2*>(lds + L_XN + (qw * 128 + 8 * i + (lane >> 3)) * 8);
#pragma unroll
        for (int c = 0; c < 4; ++c) { const f32x2 w2 = {bf_lo(wv[i][c]), bf_hi(wv[i][c])}; a0[c] += xn[0] * w2; a1[c] += xn[1] * w2; }
      }
      float t[8];
#pragma unroll
      for (int c = 0; c < 4; ++c) { t[2 * c] = red32(a0[c][0], a1[c][0]); t[2 * c + 1] = red32(a0[c][1], a1[c][1]); }
#pragma unroll
      for (int c = 0; c < 4; ++c) { t[c] = red16(t[c], t[c + 4]); t[c] += dpp<ROR8>(t[c]); }
      if ((lane & 8) == 0)
        *reinterpret_cast<f32x4*>(lds + L_Q + qw * 512 + (lane >> 5) * 256 + (8 * (lane & 7) + 4 * ((lane >> 4) & 1)) * 4) = f32x4{t[0], t[1], t[2], t[3]};
      asm volatile("s_waitcnt lgkmcnt(0)" ::: "memory");
      if (lane == 0) __hip_atomic_fetch_add((lds_u32*)(lds + L_QCNT), 1u, __ATOMIC_RELAXED, __HIP_MEMORY_SCOPE_WORKGROUP);
    }
    if (qw == 0) {
      const unsigned qhi = (unsigned)(ep >> 16), qlo = ((unsigned)ep) << 16;
      if (lane == 0) { LDS_ST(L_TAG, ((unsigned)ep) << 24); LDS_ST(L_TAG + 4, (unsigned)(ep >> 8)); LDS_ST(L_TAG + 8, qlo); LDS_ST(L_TAG + 12, qhi); LDS_ST(L_TAG + 16, (unsigned)ep); LDS_ST(L_TAG + 20, (unsigned)(ep >> 32)); }
      asm volatile("s_waitcnt lgkmcnt(0)" ::: "memory");
      if (lane == 0) LDS_ST(L_TRDY, 1u);
      if (slab) {
        while (LDS_LD(L_QCNT) != (unsigned)NQW) { }
        asm volatile("" ::: "memory");
        f32x2 qs = {0.f, 0.f};
#pragma unroll
        for (int w = 0; w < NQW; ++w) qs += *reinterpret_cast<const f32x2*>(lds + L_Q + w * 512 + lane * 8);
        unsigned pk = cvtpk(qs[0], qs[1]);
        if (!isq) pk = cvtpk(fmaxf(bf_lo(pk), 0.f), fmaxf(bf_hi(pk), 0.f));
        const unsigned dst = isq ? WS_Q + (((lane >> 5) * 8 + kvh) * 512 + r * 64 + 2 * (lane & 31)) * 8
                                 : WS_HX + ((lane >> 5) * 2048 + 64 * (4 * kvh + r - 16) + 2 * (lane & 31)) * 8;
        ST16((u32x4{qlo | (pk & 0xffffu), qhi, qlo | (pk >> 16), qhi}), dst);
      }
      STAMP(1);
      {
        const unsigned qb = WS_Q + (b * 8 + kvh) * 4096 + lane * 64;
        const u64 t0 = __builtin_amdgcn_s_memrealtime();
        if (!slab) while (__builtin_amdgcn_s_memrealtime() - t0 < (u64)QDELAY) __builtin_amdgcn_s_sleep(8);
        u32x4 qv[4];
        for (;;) {
          asm volatile("" ::: "memory");
#pragma unroll
          for (int i = 0; i < 4; ++i) qv[i] = LD16(qb + i * 16);
          unsigned bad = 0;
#pragma unroll
          for (int i = 0; i < 4; ++i) bad |= BAD16(qv[i]);
          if (__builtin_amdgcn_ballot_w64(bad != 0) == 0 || timed_out(t0)) break;
          __builtin_amdgcn_s_sleep(QSLEEP);
        }
        u32x4 f;
#pragma unroll
        for (int i = 0; i < 4; ++i) f[i] = (qv[i][0] & 0xffffu) | (qv[i][2] << 16);
        *reinterpret_cast<u32x4*>(lds + L_QF + lane * 16) = f;
        asm volatile("s_waitcnt lgkmcnt(0)" ::: "memory");
        if (lane == 0) { LDS_ST(L_QRDY, 1u); st_g(ws, WS_FLAG + bid * 8, ep); }
      }
      STAMP(2);
    } else {
      {
        const gptr wp = gwo + (size_t)(kvh * 512 + (sp >> 2) * 128 + g) * DM + (sp & 3) * 128 + 8 * n;
#pragma unroll
        for (int i = 0; i < 11; ++i) { const int q4 = min((qw - 1) * 11 + i, 31);
          wv[i] = ldnt(wp + (size_t)q4 * 4 * DM);
        }
#pragma unroll
        for (int i = 0; i < 11; ++i) { const int q4 = min((qw - 1) * 11 + i, 31); *reinterpret_cast<u32x4*>(lds + L_WO + (q4 * 4 + g) * 256 + ((n ^ (2 * (q4 & 7))) * 16)) = wv[i]; }
        asm volatile("s_waitcnt lgkmcnt(0)" ::: "memory");
      }
      if (qw != 3) return;
      while (LDS_LD(L_TRDY) == 0u) { }
      asm volatile("" ::: "memory");
      const unsigned tlo = LDS_LD(L_TAG), thi = LDS_LD(L_TAG + 4), qlo = LDS_LD(L_TAG + 8), qhi = LDS_LD(L_TAG + 12);
      if (r >= 8 && r < 16) {
        u32x4 w2v[28];
        const gptr wp = gw2 + (size_t)(224 * kvh + (lane >> 3)) * DM + 64 * (r - 8) + 8 * (lane & 7);
#pragma unroll
        for (int i = 0; i < 28; ++i) w2v[i] = ldnt(wp + (size_t)i * 8 * DM);
        const unsigned hb = WS_HX + (224 * kvh + 2 * lane) * 8;
        const u64 t0 = __builtin_amdgcn_s_memrealtime();
        u32x4 hv[4];
        for (;;) {
          asm volatile("" ::: "memory");
          hv[0] = LD16(hb); hv[1] = LD16(hb + 1024); hv[2] = LD16(hb + 16384); hv[3] = LD16(hb + 16384 + 1024);
          const unsigned bad = BAD16(hv[0]) | BAD16(hv[2]) | (lane < 48 ? BAD16(hv[1]) | BAD16(hv[3]) : 0u);
          if (__builtin_amdgcn_ballot_w64(bad != 0) == 0 || timed_out(t0)) break;
          __builtin_amdgcn_s_sleep(64);
        }
        *reinterpret_cast<f32x4*>(lds + L_XN + lane * 16) = f32x4{bf_lo(hv[0][0]), bf_lo(hv[2][0]), bf_lo(hv[0][2]), bf_lo(hv[2][2])};
        *reinterpret_cast<f32x4*>(lds + L_XN + 1024 + lane * 16) = f32x4{bf_lo(hv[1][0]), bf_lo(hv[3][0]), bf_lo(hv[1][2]), bf_lo(hv[3][2])};
        asm volatile("s_waitcnt lgkmcnt(0)" ::: "memory");
        f32x2 a0[4], a1[4];
#pragma unroll
        for (int c = 0; c < 4; ++c) { a0[c] = f32x2{0.f, 0.f}; a1[c] = f32x2{0.f, 0.f}; }
#pragma unroll
        for (int i = 0; i < 28; ++i) {
          const f32x2 hn = *reinterpret_cast<const f32x2*>(lds + L_XN + (8 * i + (lane >> 3)) * 8);
#pragma unroll
          for (int c = 0; c < 4; ++c) { const f32x2 w2 = {bf_lo(w2v[i][c]), bf_hi(w2v[i][c])}; a0[c] += hn[0] * w2; a1[c] += hn[1] * w2; }
        }
        float t[8];
#pragma unroll
        for (int c = 0; c < 4; ++c) { t[2 * c] = red32(a0[c][0], a1[c][0]); t[2 * c + 1] = red32(a0[c][1], a1[c][1]); }
#pragma unroll
        for (int c = 0; c < 4; ++c) { t[c] = red16(t[c], t[c + 4]); t[c] += dpp<ROR8>(t[c]); }
        const float y0 = (lane & 8) ? t[2] : t[0], y1 = (lane & 8) ? t[3] : t[1];
        ST16((u32x4{f24(y0, tlo), thi, f24(y1, tlo), thi}),
             WS_YX + ((kvh * 8 + r - 8) * 128 + (lane >> 5) * 64 + 8 * (lane & 7) + 4 * ((lane >> 4) & 1) + 2 * ((lane >> 3) & 1)) * 8);
      }
      STAMP(9);
      while (LDS_LD(L_PROG) == 0u) __builtin_amdgcn_s_sleep(32);
      STAMP(10);
      {
        const int jh_ = sp >> 2, jc_ = sp & 3;
        const unsigned gb_ = WS_H1 + (unsigned)(kvh + 128 * b) * OBX;
        unsigned acc_ = 0;
        acc_ ^= __builtin_amdgcn_raw_buffer_load_b32(rsrc, WS_H1 + bid * OBX + (lane < 36 ? lane : 0) * 128, 0, PRETOUCH);
#pragma unroll
        for (int i = 0; i < 3; ++i) { const int e = min(64 * i + lane, 143); acc_ ^= __builtin_amdgcn_raw_buffer_load_b32(rsrc, gb_ + (e / 9) * 8 * OBX + jh_ * 1152 + (e % 9) * 128, 0, PRETOUCH); }
        acc_ ^= __builtin_amdgcn_raw_buffer_load_b32(rsrc, lane < 32 ? WS_H2 + ((b * 32 + (cg >> 2)) * 32 + lane) * 128
                                                              : WS_H2 + ((b * 32 + jc_ * 8 + (lane & 7)) * 32 + kvh * 4 + jh_) * 128, 0, PRETOUCH);
        acc_ ^= *(const GAS unsigned*)(p.out + b * DM + 4 * cg);
        asm volatile("" :: "v"(acc_));
      }
      if (bid == 0) {
        const unsigned elo = LDS_LD(L_TAG + 16), ehi = LDS_LD(L_TAG + 20);
        const u64 t0 = __builtin_amdgcn_s_memrealtime();
        for (;;) {
          asm volatile("" ::: "memory");
          const u32x4 f0 = LD16(WS_FLAG + lane * 16), f1 = LD16(WS_FLAG + 1024 + lane * 16);
          const unsigned bad = (f0[0] ^ elo) | (f0[1] ^ ehi) | (f0[2] ^ elo) | (f0[3] ^ ehi) | (f1[0] ^ elo) | (f1[1] ^ ehi) | (f1[2] ^ elo) | (f1[3] ^ ehi);
          if (__builtin_amdgcn_ballot_w64(bad != 0) == 0 || timed_out(t0)) break;
          __builtin_amdgcn_s_sleep(32);
        }
        if (lane == 0) st_g(ws, WS_E, ((u64)ehi << 32) | elo);
      }
      {
        const unsigned yb = WS_YX + ((((lane >> 1) & 7) * 8 + (cg >> 4)) * 128 + b * 64 + (cg & 15) * 4 + 2 * (lane & 1)) * 8;
        const u64 t0 = __builtin_amdgcn_s_memrealtime();
        u32x4 yv;
        for (;;) {
          asm volatile("" ::: "memory");
          yv = LD16(yb);
          if (__builtin_amdgcn_ballot_w64(BAD24(yv) != 0) == 0 || timed_out(t0)) break;
          __builtin_amdgcn_s_sleep(32);
        }
        const float ya = rbf(row_sum8_odd_even(uf24(yv[0]))), yc = rbf(row_sum8_odd_even(uf24(yv[2])));
        if (lane < 2) *reinterpret_cast<f32x2*>(lds + L_GATE + lane * 8) = f32x2{rbf(ya / (1.f + __expf(-ya))), rbf(yc / (1.f + __expf(-yc)))};
        asm volatile("s_waitcnt lgkmcnt(0)" ::: "memory");
      }
      STAMP(5);
      STAMP_DUMP();
      return;
    }
    const unsigned thi = (unsigned)(ep >> 8), tlo = ((unsigned)ep) << 24;
    BARRIER();
    STAMP(6);
    __builtin_amdgcn_s_setprio(0);
    BARRIER();
    STAMP(11);
    __builtin_amdgcn_s_sleep(H0SLEEP);
    {
      const f32x2 gate = *reinterpret_cast<const f32x2*>(lds + L_GATE + (lane & 1) * 8);
      const unsigned ib = WS_H2 + (((b * 32 + (cg >> 2)) * 32 + (lane >> 1)) * 16 + (cg & 3) * 4 + 2 * (lane & 1)) * 8;
      const u64 t0 = __builtin_amdgcn_s_memrealtime();
      u32x4 sv = {0u, ~thi, 0u, 0u};
      for (;;) {
        asm volatile("" ::: "memory");
        if (BAD24(sv) != 0) sv = LD16(ib);
        if (__builtin_amdgcn_ballot_w64(BAD24(sv) != 0) == 0 || timed_out(t0)) break;
      }
      float s0 = row_sum8_odd_even(uf24(sv[0])); s0 = red16(s0, s0); s0 = red32(s0, s0);
      float s1 = row_sum8_odd_even(uf24(sv[2])); s1 = red16(s1, s1); s1 = red32(s1, s1);
      const float r0 = bf_lo(xo) + rbf(rbf(s0) * gate[0]), r1 = bf_hi(xo) + rbf(rbf(s1) * gate[1]);
      if (lane < 2) *(GAS unsigned*)(p.out + b * DM + 4 * cg + 2 * lane) = cvtpk(r0, r1);
      STAMP(8);
      STAMP_DUMP();
    }
    return;
  }

  {
    l = red16(l, l); l = red32(l, l);
    if (!dry) {
      *reinterpret_cast<f32x4*>(lds + L_O + tw * 2048 + g * 512 + n * 32) = o0;
      *reinterpret_cast<f32x4*>(lds + L_O + tw * 2048 + g * 512 + n * 32 + 16) = o1;
    }
    if (!dry && (lane & 0x33) == 0) {
      *reinterpret_cast<float*>(lds + L_M + tw * 16 + (n >> 2) * 4) = mc;
      *reinterpret_cast<float*>(lds + L_L + tw * 16 + (n >> 2) * 4) = l;
    }
    const unsigned tlo = LDS_LD(L_TAG), thi = LDS_LD(L_TAG + 4);
    STAMP(5);
    if (!dry) BARRIER();
    STAMP(6);
    if (tw < 4) {
      float mw[NSW], M = -INFINITY;
#pragma unroll
      for (int w = 0; w < NSW; ++w) { mw[w] = *reinterpret_cast<const float*>(lds + L_M + w * 16 + tw * 4); M = fmaxf(M, mw[w]); }
      M = asf(asu(M) & 0xffffff00u);
      float Oa = 0.f, Ob = 0.f, L = 0.f;
#pragma unroll
      for (int w = 0; w < NSW; ++w) {
        const float wt = __builtin_amdgcn_exp2f(mw[w] - M);
        const f32x2 ov = *reinterpret_cast<const f32x2*>(lds + L_O + w * 2048 + tw * 512 + lane * 8);
        Oa = fmaf(wt, ov[0], Oa); Ob = fmaf(wt, ov[1], Ob);
        L = fmaf(wt, *reinterpret_cast<const float*>(lds + L_L + w * 16 + tw * 4), L);
      }
      if (!dry) ST16((u32x4{f24(Oa, tlo), thi, f24(Ob, tlo), thi}), WS_H1 + bid * OBX + tw * 1152 + lane * 16);
      if (!dry && lane == 0) ST16((u32x4{f24(M, tlo), thi, f24(L, tlo), thi}), WS_H1 + bid * OBX + tw * 1152 + 1024);
    }
    STAMP(7);
    const int pw = tw & 3;
    f32x2 wf[2][4][4];
    if (tw >= 4) {
#pragma unroll
      for (int cb = 0; cb < 2; ++cb)
#pragma unroll
        for (int i = 0; i < 4; ++i) {
          const u32x4 wv = *reinterpret_cast<const u32x4*>(lds + L_WO + (4 * (lane >> 1) + i) * 256 + (((4 * pw + 2 * cb + (lane & 1)) ^ (lane & 14)) * 16));
#pragma unroll
          for (int c = 0; c < 4; ++c) { wf[cb][i][c] = f32x2{bf_lo(wv[c]), bf_hi(wv[c])}; asm volatile("" : "+v"(wf[cb][i][c])); }
        }
    }
    const int jh = sp >> 2, jc = sp & 3;
    {
      const unsigned gb = WS_H1 + (unsigned)(kvh + 128 * b) * OBX;
      const unsigned oa_off = gb + (2 * tw) * 8 * OBX + jh * 1152 + lane * 16, ml_off = gb + (2 * tw + (lane & 1)) * 8 * OBX + jh * 1152 + 1024;
      const u64 t0 = __builtin_amdgcn_s_memrealtime();
      u32x4 va = {0u, ~thi, 0u, 0u}, vb = va, vm = va;
      for (;;) {
        asm volatile("" ::: "memory");
        if (BAD24(va) != 0) va = LD16(oa_off);
        if (BAD24(vb) != 0) vb = LD16(oa_off + 8 * OBX);
        if (BAD24(vm) != 0) vm = LD16(ml_off);
        if (__builtin_amdgcn_ballot_w64((BAD24(va) | BAD24(vb) | BAD24(vm)) != 0) == 0 || dry || timed_out(t0)) break;
      }
      const float Ms = uf24(vm[0]), M = fmaxf(Ms, dpp<XOR1>(Ms));
      const float wt = __builtin_amdgcn_exp2f(Ms - M), wo_ = dpp<XOR1>(wt);
      const float wl = wt * uf24(vm[2]), L = wl + dpp<XOR1>(wl);
      const float wa = (lane & 1) ? wo_ : wt, wb = (lane & 1) ? wt : wo_;
      if (!dry) *reinterpret_cast<f32x2*>(lds + L_OP + tw * 512 + lane * 8) = f32x2{fmaf(wa, uf24(va[0]), wb * uf24(vb[0])), fmaf(wa, uf24(va[2]), wb * uf24(vb[2]))};
      if (!dry && lane == 0) *reinterpret_cast<f32x2*>(lds + L_ML2 + tw * 8) = f32x2{M, L};
    }
    STAMP(4);
    if (!dry) BARRIER();
    STAMP(8);
    if (tw >= 4) {
      f32x4 ml[NSW / 2], op[NSW];
#pragma unroll
      for (int w = 0; w < NSW / 2; ++w) ml[w] = *reinterpret_cast<const f32x4*>(lds + L_ML2 + w * 16);
#pragma unroll
      for (int w = 0; w < NSW; ++w) op[w] = *reinterpret_cast<const f32x4*>(lds + L_OP + w * 512 + (lane >> 1) * 16);
      __builtin_amdgcn_sched_barrier(0);
      float M = -INFINITY, L = 0.f, cw[NSW];
#pragma unroll
      for (int w = 0; w < NSW / 2; ++w) M = fmaxf(M, fmaxf(ml[w][0], ml[w][2]));
#pragma unroll
      for (int w = 0; w < NSW / 2; ++w) {
        cw[2 * w] = __builtin_amdgcn_exp2f(ml[w][0] - M); cw[2 * w + 1] = __builtin_amdgcn_exp2f(ml[w][2] - M);
        L = fmaf(cw[2 * w], ml[w][1], L); L = fmaf(cw[2 * w + 1], ml[w][3], L);
      }
      f32x4 ov = cw[0] * op[0];
#pragma unroll
      for (int w = 1; w < NSW; ++w) ov += cw[w] * op[w];
      float inv = __builtin_amdgcn_rcpf(L);
      inv = fmaf(fmaf(-L, inv, 1.0f), inv, inv);
      float o[4];
#pragma unroll
      for (int i = 0; i < 4; ++i) o[i] = rbf(ov[i] * inv);
      float e[2][2];
#pragma unroll
      for (int cb = 0; cb < 2; ++cb) {
        f32x2 ac[4];
#pragma unroll
        for (int i = 0; i < 4; ++i)
#pragma unroll
          for (int c = 0; c < 4; ++c) ac[c] = i == 0 ? o[0] * wf[cb][0][c] : ac[c] + o[i] * wf[cb][i][c];
        float acc[4];
        acc[0] = red32(ac[0][0], ac[2][0]); acc[1] = red32(ac[0][1], ac[2][1]);
        acc[2] = red32(ac[1][0], ac[3][0]); acc[3] = red32(ac[1][1], ac[3][1]);
#pragma unroll
        for (int c = 0; c < 2; ++c) e[cb][c] = row_sum8_odd_even(red16(acc[c], acc[c + 2]));
      }
      const float e0 = (lane & 2) ? e[1][0] : e[0][0], e1 = (lane & 2) ? e[1][1] : e[0][1];
      if ((lane & 12) == 0)
        ST16((u32x4{f24(e0, tlo), thi, f24(e1, tlo), thi}),
             WS_H2 + (((b * 32 + jc * 8 + 2 * pw + ((lane >> 1) & 1)) * 32 + kvh * 4 + jh) * 16 + 8 * (lane & 1) + 4 * (lane >> 5) + 2 * ((lane >> 4) & 1)) * 8);
    }
    STAMP(9);
    if (!dry) STAMP_DUMP();
  }
}

}

extern "C" void kernel_launch(void* const* d_in, const int* in_sizes, int n_in, void* d_out, int out_size, void* d_ws,
                              size_t ws_size, hipStream_t stream) {
  Params p;
  p.x = (const unsigned short*)d_in[0]; p.k = (const unsigned short*)d_in[1]; p.v = (const unsigned short*)d_in[2];
  p.wq = (const unsigned short*)d_in[3]; p.wo = (const unsigned short*)d_in[4];
  p.w1 = (const unsigned short*)d_in[6]; p.w2 = (const unsigned short*)d_in[7];
  p.out = (unsigned short*)d_out; p.ws = (unsigned char*)d_ws;
  hipLaunchKernelGGL(fused_decode, dim3(256), dim3(NTHR), 0, stream, p);
}
```

```cpp
#include <hip/hip_runtime.h>
#include <hip/hip_bf16.h>
#include <cstdint>

namespace {

typedef unsigned u32x4 __attribute__((ext_vector_type(4)));
typedef unsigned u32x2 __attribute__((ext_vector_type(2)));
typedef float f32x4 __attribute__((ext_vector_type(4)));
typedef float f32x2 __attribute__((ext_vector_type(2)));
typedef __bf16 bf16x2 __attribute__((ext_vector_type(2)));
typedef short s16x8 __attribute__((ext_vector_type(8)));
typedef short s16x4 __attribute__((ext_vector_type(4)));
typedef unsigned long long u64;
#define GAS __attribute__((address_space(1)))
typedef GAS u64 gu64;
typedef const GAS unsigned short* gptr;

constexpr int SK = 32768, DH = 128, DM = 512, DFF = 1792, HKV = 8;
constexpr int NSPLIT = 16, KEYS = SK / NSPLIT;
constexpr int NSW = 8;
constexpr int NQW = 4;
#define INFL_PRO 3
#define INFL_LO2 1
#define INFL_LO 2
#define INFL_MAIN 4
#define INFL_HI 7
constexpr int CTRL_T0 = 350, CTRL_TR = 227;
constexpr int CTRL_BAND = 40, CTRL_BAND2 = 220;
constexpr int QDELAY = 600;
constexpr int NTHR = (NSW + NQW) * 64;
constexpr int NTILE = KEYS / 16;
constexpr int W = 4;

constexpr unsigned WS_E = 0;
constexpr unsigned WS_FLAG = 0x1000;
constexpr unsigned WS_Q = 0x2000;
constexpr unsigned WS_HX = 0x12000;
constexpr unsigned WS_YX = 0x20000;
constexpr unsigned OBX = 4608;
constexpr unsigned WS_H2 = 0x30000;
constexpr unsigned WS_H1 = 0x70000;
constexpr unsigned WS_END = WS_H1 + 256 * OBX;

constexpr float C2 = 0.08838834764831845f * 1.4426950408889634f;
constexpr u64 SPIN_TICKS = 4000000ull;

constexpr int L_O = 0;
constexpr int L_M = L_O + NSW * 2048;
constexpr int L_L = L_M + NSW * 16;
constexpr int L_Q = L_L + NSW * 16;
constexpr int L_XN = L_Q + 4 * 512;
constexpr int L_QF = L_XN + 4096;
constexpr int L_OP = L_QF + 1024;
constexpr int L_ML2 = L_OP + NSW * 512;
constexpr int L_WO = L_ML2 + NSW * 8;
constexpr int L_TAG = L_WO + 128 * 256;
constexpr int L_GATE = L_TAG + 32;
constexpr int L_QCNT = L_GATE + 16;
constexpr int L_QRDY = L_QCNT + 4;
constexpr int L_PROG = L_QCNT + 8;
constexpr int L_TRDY = L_QCNT + 12;
constexpr int L_END = L_QCNT + 16;

__device__ __forceinline__ float asf(unsigned u) { return __builtin_bit_cast(float, u); }
__device__ __forceinline__ unsigned asu(float f) { return __builtin_bit_cast(unsigned, f); }
__device__ __forceinline__ float bf_lo(unsigned u) { return asf(u << 16); }
__device__ __forceinline__ float bf_hi(unsigned u) { return asf(u & 0xffff0000u); }
__device__ __forceinline__ unsigned cvtpk(float lo, float hi) {
  const f32x2 v = {lo, hi};
  return __builtin_bit_cast(unsigned, __builtin_convertvector(v, bf16x2));
}
__device__ __forceinline__ float rbf(float x) { return bf_lo(cvtpk(x, x)); }
template <int CTRL> __device__ __forceinline__ float dpp(float x) {
  return asf(__builtin_amdgcn_mov_dpp((int)asu(x), CTRL, 0xf, 0xf, true));
}
constexpr int XOR1 = 0xB1, XOR2 = 0x4E, HMIRR = 0x141, MIRR = 0x140, ROR4 = 0x124, ROR8 = 0x128;
__device__ __forceinline__ float red32(float a, float b) {
  auto r = __builtin_amdgcn_permlane32_swap(asu(a), asu(b), false, false);
  return asf((unsigned)r[0]) + asf((unsigned)r[1]);
}
__device__ __forceinline__ float red16(float a, float b) {
  auto r = __builtin_amdgcn_permlane16_swap(asu(a), asu(b), false, false);
  return asf((unsigned)r[0]) + asf((unsigned)r[1]);
}
__device__ __forceinline__ float max_x16_x32(float x) {
  auto s = __builtin_amdgcn_permlane16_swap(asu(x), asu(x), false, false);
  x = fmaxf(asf((unsigned)s[0]), asf((unsigned)s[1]));
  auto t = __builtin_amdgcn_permlane32_swap(asu(x), asu(x), false, false);
  return fmaxf(asf((unsigned)t[0]), asf((unsigned)t[1]));
}
__device__ __forceinline__ float row_sum16(float x) {
  x += dpp<XOR1>(x); x += dpp<XOR2>(x); x += dpp<HMIRR>(x); x += dpp<MIRR>(x); return x;
}
__device__ __forceinline__ float row_sum8_odd_even(float t) {
  t += dpp<ROR8>(t); t += dpp<ROR4>(t); t += dpp<XOR2>(t); return t;
}
__device__ __forceinline__ float wave_sum(float x) { x = row_sum16(x); x = red16(x, x); return red32(x, x); }
__device__ __forceinline__ u32x4 ldnt(gptr p) { return __builtin_nontemporal_load((const GAS u32x4*)p); }
__device__ __forceinline__ u32x4 ld16(gptr p) { return *(const GAS u32x4*)p; }
__device__ __forceinline__ void st_g(unsigned char* ws, unsigned off, u64 v) {
  __hip_atomic_store((gu64*)(ws + off), v, __ATOMIC_RELAXED, __HIP_MEMORY_SCOPE_AGENT);
}
__device__ __forceinline__ u64 ld_g(unsigned char* ws, unsigned off) {
  return __hip_atomic_load((gu64*)(ws + off), __ATOMIC_RELAXED, __HIP_MEMORY_SCOPE_AGENT);
}
__device__ __forceinline__ unsigned f24(float x, unsigned tlo) { const unsigned u = asu(x); return ((u + 0x7fu + ((u >> 8) & 1u)) >> 8) | tlo; }
__device__ __forceinline__ float uf24(unsigned lo) { return asf(lo << 8); }
#define BAD24(v) (((v)[1] ^ thi) | ((v)[3] ^ thi) | (((v)[0] ^ tlo) >> 24) | (((v)[2] ^ tlo) >> 24))
#define BAD16(v) (((v)[1] ^ qhi) | ((v)[3] ^ qhi) | (((v)[0] ^ qlo) >> 16) | (((v)[2] ^ qlo) >> 16))
#define LD16(off) __builtin_amdgcn_raw_buffer_load_b128(rsrc, (off), 0, 16)
#define ST16(v, off) __builtin_amdgcn_raw_buffer_store_b128((v), rsrc, (off), 0, 16)
__device__ __forceinline__ bool timed_out(u64 t0) { return __builtin_amdgcn_s_memrealtime() - t0 > SPIN_TICKS; }
#define BARRIER() do { asm volatile("s_waitcnt lgkmcnt(0)" ::: "memory"); __builtin_amdgcn_s_barrier(); asm volatile("" ::: "memory"); } while (0)
typedef __attribute__((address_space(3))) unsigned lds_u32;
#define LDS_LD(off) __hip_atomic_load((lds_u32*)(lds + (off)), __ATOMIC_RELAXED, __HIP_MEMORY_SCOPE_WORKGROUP)
#define LDS_ST(off, v) __hip_atomic_store((lds_u32*)(lds + (off)), (v), __ATOMIC_RELAXED, __HIP_MEMORY_SCOPE_WORKGROUP)

__device__ __forceinline__ void ln_stats(u32x4 xv, float& mb, float& sd) {
  float e[8];
#pragma unroll
  for (int i = 0; i < 4; ++i) { e[2 * i] = bf_lo(xv[i]); e[2 * i + 1] = bf_hi(xv[i]); }
  float s = ((e[0] + e[1]) + (e[2] + e[3])) + ((e[4] + e[5]) + (e[6] + e[7]));
  const float mean = wave_sum(s) * (1.0f / DM);
  float q = 0.f;
#pragma unroll
  for (int i = 0; i < 8; ++i) { const float d = e[i] - mean; q = fmaf(d, d, q); }
  const float var = wave_sum(q) * (1.0f / DM);
  mb = rbf(mean);
  sd = rbf(sqrtf(rbf(rbf(var) + 1e-5f)));
}
__device__ __forceinline__ float ln_apply(float x, float mb, float sd) { return rbf(rbf(x - mb) / sd); }

struct Params {
  const unsigned short* x; const unsigned short* k; const unsigned short* v; const unsigned short* wq;
  const unsigned short* wo; const unsigned short* w1; const unsigned short* w2; unsigned short* out; unsigned char* ws;
};

__global__ void __launch_bounds__(NTHR, 3) fused_decode(Params p) {
  __shared__ __attribute__((aligned(16))) unsigned char lds[L_END];
  asm volatile("" : "+s"(p.x), "+s"(p.k), "+s"(p.v), "+s"(p.wq), "+s"(p.wo), "+s"(p.w1), "+s"(p.w2), "+s"(p.out), "+s"(p.ws));
  const gptr gx = (gptr)p.x, gk = (gptr)p.k, gv = (gptr)p.v, gwq = (gptr)p.wq, gwo = (gptr)p.wo, gw1 = (gptr)p.w1, gw2 = (gptr)p.w2;
  const int tid = threadIdx.x, lane = tid & 63;
  const int wave = __builtin_amdgcn_readfirstlane(tid >> 6);
  const int bid = blockIdx.x, kvh = bid & 7, r = bid >> 3, b = r >> 4, sp = r & 15;
  unsigned char* ws = p.ws;
  const auto rsrc = __builtin_amdgcn_make_buffer_rsrc(ws, 0, WS_END, 0x00020000);
  const int n = lane & 15, g = lane >> 4;
  const int cg = kvh * 16 + sp;

  float mc = 0.f, l = 0.f;
  f32x4 o0 = {0.f, 0.f, 0.f, 0.f}, o1 = {0.f, 0.f, 0.f, 0.f};

  if (wave < NSW) {
    const u64 t_start = __builtin_amdgcn_s_memrealtime();
    const size_t kvbase = ((size_t)(b * HKV + kvh) * SK + (size_t)sp * KEYS) * DH;
    const gptr kp = gk + kvbase + (size_t)(n >> 2) * DH + 8 * (4 * g + (n & 3));
    const gptr vp = gv + kvbase + 8 * lane;
    const int rot = ((bid * 5 + (bid >> 3) * 11) & 15) * 8;
    u32x4 kr[W][4], vr[W][4];
#pragma unroll
    for (int t = 0; t < W; ++t)
#pragma unroll
      for (int i = 0; i < 4; ++i) { kr[t][i] = u32x4{0u, 0u, 0u, 0u}; vr[t][i] = u32x4{0u, 0u, 0u, 0u}; }
#define STR2(x) #x
#define STR(x) STR2(x)
#define LDKV(dst, ptr, off, n) asm volatile("s_waitcnt vmcnt(" STR(n) ")\n\tglobal_load_dwordx4 %0, %1, off offset:" STR(off) " nt" : "+v"(dst) : "v"(ptr) : "memory")
#define ISSUE(slot, kq_, vq_, n) do { \
      LDKV(kr[slot][0], kq_, 0, n); LDKV(kr[slot][1], kq_, 1024, n); LDKV(kr[slot][2], kq_, 2048, n); LDKV(kr[slot][3], kq_, 3072, n); \
      LDKV(vr[slot][0], vq_, 0, n); LDKV(vr[slot][1], vq_, 1024, n); LDKV(vr[slot][2], vq_, 2048, n); LDKV(vr[slot][3], vq_, 3072, n); \
      __builtin_amdgcn_sched_barrier(0); } while (0)
    __builtin_amdgcn_s_barrier();
    __builtin_amdgcn_sched_barrier(0);
#pragma unroll
    for (int t = 0; t < W; ++t) {
      const size_t o = (size_t)((t * NSW + wave + rot) & (NTILE - 1)) * (16 * DH);
      const gptr kq = kp + o;
      const gptr vq = vp + o;
      ISSUE(t, kq, vq, INFL_PRO);
    }
    unsigned nxt = W * NSW + wave;
    while (LDS_LD(L_QRDY) == 0u) __builtin_amdgcn_s_sleep(1);
    asm volatile("" ::: "memory");
    const s16x8 qf = *reinterpret_cast<const s16x8*>(lds + L_QF + ((n >> 2) * 16 + 4 * g + (n & 3)) * 16);
    const unsigned jm = n & 3;
    const unsigned mask0 = jm == 0 ? 0x0000ffffu : (jm == 1 ? 0xffff0000u : 0u);
    const unsigned mask1 = jm == 2 ? 0x0000ffffu : (jm == 3 ? 0xffff0000u : 0u);
    const bool sel1 = (lane & 1) != 0, sel2 = (lane & 2) != 0;
    float m = -INFINITY;

#define TILE(slot) do { \
      asm volatile("" : "+v"(kr[slot][0]), "+v"(kr[slot][1]), "+v"(kr[slot][2]), "+v"(kr[slot][3]), \
                        "+v"(vr[slot][0]), "+v"(vr[slot][1]), "+v"(vr[slot][2]), "+v"(vr[slot][3])); \
      float s_[4]; \
      _Pragma("unroll") for (int i_ = 0; i_ < 4; ++i_) { \
        const f32x4 d_ = __builtin_amdgcn_mfma_f32_16x16x32_bf16(__builtin_bit_cast(s16x8, kr[slot][i_]), qf, f32x4{0.f, 0.f, 0.f, 0.f}, 0, 0, 0); \
        const float t0_ = sel1 ? d_[1] : d_[0], t1_ = sel1 ? d_[3] : d_[2]; \
        float u_ = sel2 ? t1_ : t0_; u_ += dpp<XOR1>(u_); u_ += dpp<XOR2>(u_); s_[i_] = u_; } \
      const float mx_ = fmaxf(fmaxf(s_[0], s_[1]), fmaxf(s_[2], s_[3])); \
      if (__builtin_amdgcn_ballot_w64(mx_ > m) != 0) { \
        const float mn_ = fmaxf(m, max_x16_x32(mx_)); \
        const float mcn_ = mn_ * C2; \
        const float al_ = (m == -INFINITY) ? 0.f : __builtin_amdgcn_exp2f(mc - mcn_); \
        l *= al_; m = mn_; mc = mcn_; \
        const float a0_ = asf(__builtin_amdgcn_readlane((int)asu(al_), 0)); \
        const float a1_ = asf(__builtin_amdgcn_readlane((int)asu(al_), 4)); \
        const float a2_ = asf(__builtin_amdgcn_readlane((int)asu(al_), 8)); \
        const float a3_ = asf(__builtin_amdgcn_readlane((int)asu(al_), 12)); \
        const float ao_ = g == 0 ? a0_ : (g == 1 ? a1_ : (g == 2 ? a2_ : a3_)); \
        o0 *= ao_; o1 *= ao_; } \
      _Pragma("unroll") for (int i_ = 0; i_ < 4; ++i_) { \
        const float p_ = __builtin_amdgcn_exp2f(fmaf(s_[i_], C2, -mc)); \
        l += p_; \
        const unsigned pb_ = cvtpk(p_, p_); \
        const u32x2 a_ = {pb_ & mask0, pb_ & mask1}; \
        const u32x2 va_ = {vr[slot][i_][0], vr[slot][i_][1]}, vb_ = {vr[slot][i_][2], vr[slot][i_][3]}; \
        o0 = __builtin_amdgcn_mfma_f32_16x16x16bf16_1k(__builtin_bit_cast(s16x4, a_), __builtin_bit_cast(s16x4, va_), o0, 0, 0, 0); \
        o1 = __builtin_amdgcn_mfma_f32_16x16x16bf16_1k(__builtin_bit_cast(s16x4, a_), __builtin_bit_cast(s16x4, vb_), o1, 0, 0, 0); } \
      asm volatile("" : "+v"(o0), "+v"(o1)); \
    } while (0)

    int left = W;
#pragma unroll 1
    for (;;) {
#pragma unroll
      for (int s = 0; s < W; ++s) {
        TILE(s);
        __builtin_amdgcn_sched_barrier(0);
        const unsigned cur = __builtin_amdgcn_readfirstlane(nxt);
        if (cur < (unsigned)NTILE) {
          nxt = cur + NSW;
          if (cur == (unsigned)(NTILE - 40) && lane == 0) LDS_ST(L_PROG, 1u);
          const size_t o = (size_t)((cur + rot) & (NTILE - 1)) * (16 * DH);
          const gptr kq = kp + o;
          const gptr vq = vp + o;
          const int lagt = (int)(unsigned)(__builtin_amdgcn_s_memrealtime() - t_start) - (CTRL_T0 + (int)(cur >> 3) * CTRL_TR);
          const int mode = __builtin_amdgcn_readfirstlane(lagt > CTRL_BAND ? 3 : (lagt > -CTRL_BAND ? 2 : (lagt > -CTRL_BAND2 ? 1 : 0)));
#define LDKV2(dst, ptr, off) asm volatile("s_cmp_ge_u32 %2, 3\n\ts_cbranch_scc1 3f\n\ts_cmp_eq_u32 %2, 2\n\ts_cbranch_scc1 2f\n\ts_cmp_eq_u32 %2, 1\n\ts_cbranch_scc1 1f\n\ts_waitcnt vmcnt(" STR(INFL_LO2) ")\n1:\n\ts_waitcnt vmcnt(" STR(INFL_LO) ")\n2:\n\ts_waitcnt vmcnt(" STR(INFL_MAIN) ")\n3:\n\ts_waitcnt vmcnt(" STR(INFL_HI) ")\n\tglobal_load_dwordx4 %0, %1, off offset:" STR(off) " nt" : "+v"(dst) : "v"(ptr), "s"(mode) : "memory", "scc")
          LDKV2(kr[s][0], kq, 0); LDKV2(kr[s][1], kq, 1024); LDKV2(kr[s][2], kq, 2048); LDKV2(kr[s][3], kq, 3072);
          LDKV2(vr[s][0], vq, 0); LDKV2(vr[s][1], vq, 1024); LDKV2(vr[s][2], vq, 2048); LDKV2(vr[s][3], vq, 3072);
          __builtin_amdgcn_sched_barrier(0);
        } else {
          if (--left == 0) goto stream_done;
          if (left == 1) asm volatile("s_waitcnt vmcnt(0)" ::: "memory");
          __builtin_amdgcn_sched_barrier(0);
        }
      }
    }
  stream_done:
    ;
#undef TILE
#undef ISSUE
#undef LDKV
#undef LDKV2
  } else {
    __builtin_amdgcn_s_setprio(3);
    const int qw = wave - NSW;
    const bool isq = r < 8, isf1 = r >= 16 && r < 20 && kvh * 4 + r - 16 < 28, slab = isq || isf1;
    u32x4 wv[16];
    if (slab) {
      const int ld = isq ? 4096 : DFF;
      const gptr wp = (isq ? gwq + 64 * (8 * kvh + r) : gw1 + 64 * (4 * kvh + r - 16)) + (size_t)(128 * qw + (lane >> 3)) * ld + 8 * (lane & 7);
#pragma unroll
      for (int i = 0; i < 16; ++i) wv[i] = ldnt(wp + (size_t)i * 8 * ld);
    }
    const u32x4 xa = ld16(gx + 8 * lane);
    const u32x4 xb = ld16(gx + DM + 8 * lane);
    unsigned short xh[4];
#pragma unroll
    for (int i = 0; i < 2; ++i) { xh[2 * i] = gx[qw * 128 + 64 * i + lane]; xh[2 * i + 1] = gx[DM + qw * 128 + 64 * i + lane]; }
    u64 ep = 0; unsigned xo = 0;
    if (qw == 0) {
      ep = ld_g(ws, WS_E);
      xo = *(const GAS unsigned*)(gx + b * DM + 4 * cg + 2 * (lane & 1));
      if (lane == 0) { LDS_ST(L_QCNT, 0u); LDS_ST(L_QRDY, 0u); LDS_ST(L_PROG, 0u); LDS_ST(L_TRDY, 0u); }
    }
    __builtin_amdgcn_sched_barrier(0);
    BARRIER();
    __builtin_amdgcn_sched_barrier(0);
    ep += 1ull;
    if (slab) {
      float m0, s0, m1, s1;
      ln_stats(xa, m0, s0); ln_stats(xb, m1, s1);
#pragma unroll
      for (int i = 0; i < 2; ++i)
        *reinterpret_cast<f32x2*>(lds + L_XN + (qw * 128 + 64 * i + lane) * 8) = f32x2{ln_apply(bf_lo(xh[2 * i]), m0, s0), ln_apply(bf_lo(xh[2 * i + 1]), m1, s1)};
      asm volatile("s_waitcnt lgkmcnt(0)" ::: "memory");
      f32x2 a0[4], a1[4];
#pragma unroll
      for (int c = 0; c < 4; ++c) { a0[c] = f32x2{0.f, 0.f}; a1[c] = f32x2{0.f, 0.f}; }
#pragma unroll
      for (int i = 0; i < 16; ++i) {
        const f32x2 xn = *reinterpret_cast<const f32x2*>(lds + L_XN + (qw * 128 + 8 * i + (lane >> 3)) * 8);
#pragma unroll
        for (int c = 0; c < 4; ++c) { const f32x2 w2 = {bf_lo(wv[i][c]), bf_hi(wv[i][c])}; a0[c] += xn[0] * w2; a1[c] += xn[1] * w2; }
      }
      float t[8];
#pragma unroll
      for (int c = 0; c < 4; ++c) { t[2 * c] = red32(a0[c][0], a1[c][0]); t[2 * c + 1] = red32(a0[c][1], a1[c][1]); }
#pragma unroll
      for (int c = 0; c < 4; ++c) { t[c] = red16(t[c], t[c + 4]); t[c] += dpp<ROR8>(t[c]); }
      if ((lane & 8) == 0)
        *reinterpret_cast<f32x4*>(lds + L_Q + qw * 512 + (lane >> 5) * 256 + (8 * (lane & 7) + 4 * ((lane >> 4) & 1)) * 4) = f32x4{t[0], t[1], t[2], t[3]};
      asm volatile("s_waitcnt lgkmcnt(0)" ::: "memory");
      if (lane == 0) __hip_atomic_fetch_add((lds_u32*)(lds + L_QCNT), 1u, __ATOMIC_RELAXED, __HIP_MEMORY_SCOPE_WORKGROUP);
    }
    if (qw == 0) {
      const unsigned qhi = (unsigned)(ep >> 16), qlo = ((unsigned)ep) << 16;
      if (lane == 0) { LDS_ST(L_TAG, ((unsigned)ep) << 24); LDS_ST(L_TAG + 4, (unsigned)(ep >> 8)); LDS_ST(L_TAG + 8, qlo); LDS_ST(L_TAG + 12, qhi); LDS_ST(L_TAG + 16, (unsigned)ep); LDS_ST(L_TAG + 20, (unsigned)(ep >> 32)); }
      asm volatile("s_waitcnt lgkmcnt(0)" ::: "memory");
      if (lane == 0) LDS_ST(L_TRDY, 1u);
      if (slab) {
        while (LDS_LD(L_QCNT) != (unsigned)NQW) { }
        asm volatile("" ::: "memory");
        f32x2 qs = {0.f, 0.f};
#pragma unroll
        for (int w = 0; w < NQW; ++w) qs += *reinterpret_cast<const f32x2*>(lds + L_Q + w * 512 + lane * 8);
        unsigned pk = cvtpk(qs[0], qs[1]);
        if (!isq) pk = cvtpk(fmaxf(bf_lo(pk), 0.f), fmaxf(bf_hi(pk), 0.f));
        const unsigned dst = isq ? WS_Q + (((lane >> 5) * 8 + kvh) * 512 + r * 64 + 2 * (lane & 31)) * 8
                                 : WS_HX + ((lane >> 5) * 2048 + 64 * (4 * kvh + r - 16) + 2 * (lane & 31)) * 8;
        ST16((u32x4{qlo | (pk & 0xffffu), qhi, qlo | (pk >> 16), qhi}), dst);
      }
      {
        const unsigned qb = WS_Q + (b * 8 + kvh) * 4096 + lane * 64;
        const u64 t0 = __builtin_amdgcn_s_memrealtime();
        if (!slab) while (__builtin_amdgcn_s_memrealtime() - t0 < (u64)QDELAY) __builtin_amdgcn_s_sleep(8);
        u32x4 qv[4];
        for (;;) {
          asm volatile("" ::: "memory");
#pragma unroll
          for (int i = 0; i < 4; ++i) qv[i] = LD16(qb + i * 16);
          unsigned bad = 0;
#pragma unroll
          for (int i = 0; i < 4; ++i) bad |= BAD16(qv[i]);
          if (__builtin_amdgcn_ballot_w64(bad != 0) == 0 || timed_out(t0)) break;
          __builtin_amdgcn_s_sleep(4);
        }
        u32x4 f;
#pragma unroll
        for (int i = 0; i < 4; ++i) f[i] = (qv[i][0] & 0xffffu) | (qv[i][2] << 16);
        *reinterpret_cast<u32x4*>(lds + L_QF + lane * 16) = f;
        asm volatile("s_waitcnt lgkmcnt(0)" ::: "memory");
        if (lane == 0) { LDS_ST(L_QRDY, 1u); st_g(ws, WS_FLAG + bid * 8, ep); }
      }
    } else {
      {
        const gptr wp = gwo + (size_t)(kvh * 512 + (sp >> 2) * 128 + g) * DM + (sp & 3) * 128 + 8 * n;
#pragma unroll
        for (int i = 0; i < 11; ++i) { const int q4 = min((qw - 1) * 11 + i, 31); wv[i] = ldnt(wp + (size_t)q4 * 4 * DM); }
#pragma unroll
        for (int i = 0; i < 11; ++i) { const int q4 = min((qw - 1) * 11 + i, 31); *reinterpret_cast<u32x4*>(lds + L_WO + (q4 * 4 + g) * 256 + ((n ^ (2 * (q4 & 7))) * 16)) = wv[i]; }
        asm volatile("s_waitcnt lgkmcnt(0)" ::: "memory");
      }
      if (qw != 3) return;
      while (LDS_LD(L_TRDY) == 0u) { }
      asm volatile("" ::: "memory");
      const unsigned tlo = LDS_LD(L_TAG), thi = LDS_LD(L_TAG + 4), qlo = LDS_LD(L_TAG + 8), qhi = LDS_LD(L_TAG + 12);
      if (r >= 8 && r < 16) {
        u32x4 w2v[28];
        const gptr wp = gw2 + (size_t)(224 * kvh + (lane >> 3)) * DM + 64 * (r - 8) + 8 * (lane & 7);
#pragma unroll
        for (int i = 0; i < 28; ++i) w2v[i] = ldnt(wp + (size_t)i * 8 * DM);
        const unsigned hb = WS_HX + (224 * kvh + 2 * lane) * 8;
        const u64 t0 = __builtin_amdgcn_s_memrealtime();
        u32x4 hv[4];
        for (;;) {
          asm volatile("" ::: "memory");
          hv[0] = LD16(hb); hv[1] = LD16(hb + 1024); hv[2] = LD16(hb + 16384); hv[3] = LD16(hb + 16384 + 1024);
          const unsigned bad = BAD16(hv[0]) | BAD16(hv[2]) | (lane < 48 ? BAD16(hv[1]) | BAD16(hv[3]) : 0u);
          if (__builtin_amdgcn_ballot_w64(bad != 0) == 0 || timed_out(t0)) break;
          __builtin_amdgcn_s_sleep(64);
        }
        *reinterpret_cast<f32x4*>(lds + L_XN + lane * 16) = f32x4{bf_lo(hv[0][0]), bf_lo(hv[2][0]), bf_lo(hv[0][2]), bf_lo(hv[2][2])};
        *reinterpret_cast<f32x4*>(lds + L_XN + 1024 + lane * 16) = f32x4{bf_lo(hv[1][0]), bf_lo(hv[3][0]), bf_lo(hv[1][2]), bf_lo(hv[3][2])};
        asm volatile("s_waitcnt lgkmcnt(0)" ::: "memory");
        f32x2 a0[4], a1[4];
#pragma unroll
        for (int c = 0; c < 4; ++c) { a0[c] = f32x2{0.f, 0.f}; a1[c] = f32x2{0.f, 0.f}; }
#pragma unroll
        for (int i = 0; i < 28; ++i) {
          const f32x2 hn = *reinterpret_cast<const f32x2*>(lds + L_XN + (8 * i + (lane >> 3)) * 8);
#pragma unroll
          for (int c = 0; c < 4; ++c) { const f32x2 w2 = {bf_lo(w2v[i][c]), bf_hi(w2v[i][c])}; a0[c] += hn[0] * w2; a1[c] += hn[1] * w2; }
        }
        float t[8];
#pragma unroll
        for (int c = 0; c < 4; ++c) { t[2 * c] = red32(a0[c][0], a1[c][0]); t[2 * c + 1] = red32(a0[c][1], a1[c][1]); }
#pragma unroll
        for (int c = 0; c < 4; ++c) { t[c] = red16(t[c], t[c + 4]); t[c] += dpp<ROR8>(t[c]); }
        const float y0 = (lane & 8) ? t[2] : t[0], y1 = (lane & 8) ? t[3] : t[1];
        ST16((u32x4{f24(y0, tlo), thi, f24(y1, tlo), thi}),
             WS_YX + ((kvh * 8 + r - 8) * 128 + (lane >> 5) * 64 + 8 * (lane & 7) + 4 * ((lane >> 4) & 1) + 2 * ((lane >> 3) & 1)) * 8);
      }
      while (LDS_LD(L_PROG) == 0u) __builtin_amdgcn_s_sleep(32);
      {
        const int jh_ = sp >> 2, jc_ = sp & 3;
        const unsigned gb_ = WS_H1 + (unsigned)(kvh + 128 * b) * OBX;
        unsigned acc_ = 0;
        acc_ ^= __builtin_amdgcn_raw_buffer_load_b32(rsrc, WS_H1 + bid * OBX + (lane < 36 ? lane : 0) * 128, 0, 16);
#pragma unroll
        for (int i = 0; i < 3; ++i) { const int e = min(64 * i + lane, 143); acc_ ^= __builtin_amdgcn_raw_buffer_load_b32(rsrc, gb_ + (e / 9) * 8 * OBX + jh_ * 1152 + (e % 9) * 128, 0, 16); }
        acc_ ^= __builtin_amdgcn_raw_buffer_load_b32(rsrc, lane < 32 ? WS_H2 + ((b * 32 + (cg >> 2)) * 32 + lane) * 128
                                                              : WS_H2 + ((b * 32 + jc_ * 8 + (lane & 7)) * 32 + kvh * 4 + jh_) * 128, 0, 16);
        acc_ ^= *(const GAS unsigned*)(p.out + b * DM + 4 * cg);
        asm volatile("" :: "v"(acc_));
      }
      if (bid == 0) {
        const unsigned elo = LDS_LD(L_TAG + 16), ehi = LDS_LD(L_TAG + 20);
        const u64 t0 = __builtin_amdgcn_s_memrealtime();
        for (;;) {
          asm volatile("" ::: "memory");
          const u32x4 f0 = LD16(WS_FLAG + lane * 16), f1 = LD16(WS_FLAG + 1024 + lane * 16);
          const unsigned bad = (f0[0] ^ elo) | (f0[1] ^ ehi) | (f0[2] ^ elo) | (f0[3] ^ ehi) | (f1[0] ^ elo) | (f1[1] ^ ehi) | (f1[2] ^ elo) | (f1[3] ^ ehi);
          if (__builtin_amdgcn_ballot_w64(bad != 0) == 0 || timed_out(t0)) break;
          __builtin_amdgcn_s_sleep(32);
        }
        if (lane == 0) st_g(ws, WS_E, ((u64)ehi << 32) | elo);
      }
      {
        const unsigned yb = WS_YX + ((((lane >> 1) & 7) * 8 + (cg >> 4)) * 128 + b * 64 + (cg & 15) * 4 + 2 * (lane & 1)) * 8;
        const u64 t0 = __builtin_amdgcn_s_memrealtime();
        u32x4 yv;
        for (;;) {
          asm volatile("" ::: "memory");
          yv = LD16(yb);
          if (__builtin_amdgcn_ballot_w64(BAD24(yv) != 0) == 0 || timed_out(t0)) break;
          __builtin_amdgcn_s_sleep(32);
        }
        const float ya = rbf(row_sum8_odd_even(uf24(yv[0]))), yc = rbf(row_sum8_odd_even(uf24(yv[2])));
        if (lane < 2) *reinterpret_cast<f32x2*>(lds + L_GATE + lane * 8) = f32x2{rbf(ya / (1.f + __expf(-ya))), rbf(yc / (1.f + __expf(-yc)))};
        asm volatile("s_waitcnt lgkmcnt(0)" ::: "memory");
      }
      return;
    }
    const unsigned thi = (unsigned)(ep >> 8), tlo = ((unsigned)ep) << 24;
    BARRIER();
    __builtin_amdgcn_s_setprio(0);
    BARRIER();
    __builtin_amdgcn_s_sleep(4);
    {
      const f32x2 gate = *reinterpret_cast<const f32x2*>(lds + L_GATE + (lane & 1) * 8);
      const unsigned ib = WS_H2 + (((b * 32 + (cg >> 2)) * 32 + (lane >> 1)) * 16 + (cg & 3) * 4 + 2 * (lane & 1)) * 8;
      const u64 t0 = __builtin_amdgcn_s_memrealtime();
      u32x4 sv = {0u, ~thi, 0u, 0u};
      for (;;) {
        asm volatile("" ::: "memory");
        if (BAD24(sv) != 0) sv = LD16(ib);
        if (__builtin_amdgcn_ballot_w64(BAD24(sv) != 0) == 0 || timed_out(t0)) break;
      }
      float s0 = row_sum8_odd_even(uf24(sv[0])); s0 = red16(s0, s0); s0 = red32(s0, s0);
      float s1 = row_sum8_odd_even(uf24(sv[2])); s1 = red16(s1, s1); s1 = red32(s1, s1);
      const float r0 = bf_lo(xo) + rbf(rbf(s0) * gate[0]), r1 = bf_hi(xo) + rbf(rbf(s1) * gate[1]);
      if (lane < 2) *(GAS unsigned*)(p.out + b * DM + 4 * cg + 2 * lane) = cvtpk(r0, r1);
    }
    return;
  }

  {
    l = red16(l, l); l = red32(l, l);
    *reinterpret_cast<f32x4*>(lds + L_O + wave * 2048 + g * 512 + n * 32) = o0;
    *reinterpret_cast<f32x4*>(lds + L_O + wave * 2048 + g * 512 + n * 32 + 16) = o1;
    if ((lane & 0x33) == 0) {
      *reinterpret_cast<float*>(lds + L_M + wave * 16 + (n >> 2) * 4) = mc;
      *reinterpret_cast<float*>(lds + L_L + wave * 16 + (n >> 2) * 4) = l;
    }
    const unsigned tlo = LDS_LD(L_TAG), thi = LDS_LD(L_TAG + 4);
    BARRIER();
    if (wave < 4) {
      float mw[NSW], M = -INFINITY;
#pragma unroll
      for (int w = 0; w < NSW; ++w) { mw[w] = *reinterpret_cast<const float*>(lds + L_M + w * 16 + wave * 4); M = fmaxf(M, mw[w]); }
      M = asf(asu(M) & 0xffffff00u);
      float Oa = 0.f, Ob = 0.f, L = 0.f;
#pragma unroll
      for (int w = 0; w < NSW; ++w) {
        const float wt = __builtin_amdgcn_exp2f(mw[w] - M);
        const f32x2 ov = *reinterpret_cast<const f32x2*>(lds + L_O + w * 2048 + wave * 512 + lane * 8);
        Oa = fmaf(wt, ov[0], Oa); Ob = fmaf(wt, ov[1], Ob);
        L = fmaf(wt, *reinterpret_cast<const float*>(lds + L_L + w * 16 + wave * 4), L);
      }
      ST16((u32x4{f24(Oa, tlo), thi, f24(Ob, tlo), thi}), WS_H1 + bid * OBX + wave * 1152 + lane * 16);
      if (lane == 0) ST16((u32x4{f24(M, tlo), thi, f24(L, tlo), thi}), WS_H1 + bid * OBX + wave * 1152 + 1024);
    }
    const int pw = wave & 3;
    f32x2 wf[2][4][4];
    if (wave >= 4) {
#pragma unroll
      for (int cb = 0; cb < 2; ++cb)
#pragma unroll
        for (int i = 0; i < 4; ++i) {
          const u32x4 wv = *reinterpret_cast<const u32x4*>(lds + L_WO + (4 * (lane >> 1) + i) * 256 + (((4 * pw + 2 * cb + (lane & 1)) ^ (lane & 14)) * 16));
#pragma unroll
          for (int c = 0; c < 4; ++c) { wf[cb][i][c] = f32x2{bf_lo(wv[c]), bf_hi(wv[c])}; asm volatile("" : "+v"(wf[cb][i][c])); }
        }
    }
    const int jh = sp >> 2, jc = sp & 3;
    {
      const unsigned gb = WS_H1 + (unsigned)(kvh + 128 * b) * OBX;
      const unsigned oa_off = gb + (2 * wave) * 8 * OBX + jh * 1152 + lane * 16, ml_off = gb + (2 * wave + (lane & 1)) * 8 * OBX + jh * 1152 + 1024;
      const u64 t0 = __builtin_amdgcn_s_memrealtime();
      u32x4 va = {0u, ~thi, 0u, 0u}, vb = va, vm = va;
      for (;;) {
        asm volatile("" ::: "memory");
        if (BAD24(va) != 0) va = LD16(oa_off);
        if (BAD24(vb) != 0) vb = LD16(oa_off + 8 * OBX);
        if (BAD24(vm) != 0) vm = LD16(ml_off);
        if (__builtin_amdgcn_ballot_w64((BAD24(va) | BAD24(vb) | BAD24(vm)) != 0) == 0 || timed_out(t0)) break;
      }
      const float Ms = uf24(vm[0]), M = fmaxf(Ms, dpp<XOR1>(Ms));
      const float wt = __builtin_amdgcn_exp2f(Ms - M), wo_ = dpp<XOR1>(wt);
      const float wl = wt * uf24(vm[2]), L = wl + dpp<XOR1>(wl);
      const float wa = (lane & 1) ? wo_ : wt, wb = (lane & 1) ? wt : wo_;
      *reinterpret_cast<f32x2*>(lds + L_OP + wave * 512 + lane * 8) = f32x2{fmaf(wa, uf24(va[0]), wb * uf24(vb[0])), fmaf(wa, uf24(va[2]), wb * uf24(vb[2]))};
      if (lane == 0) *reinterpret_cast<f32x2*>(lds + L_ML2 + wave * 8) = f32x2{M, L};
    }
    BARRIER();
    if (wave >= 4) {
      f32x4 ml[NSW / 2], op[NSW];
#pragma unroll
      for (int w = 0; w < NSW / 2; ++w) ml[w] = *reinterpret_cast<const f32x4*>(lds + L_ML2 + w * 16);
#pragma unroll
      for (int w = 0; w < NSW; ++w) op[w] = *reinterpret_cast<const f32x4*>(lds + L_OP + w * 512 + (lane >> 1) * 16);
      __builtin_amdgcn_sched_barrier(0);
      float M = -INFINITY, L = 0.f, cw[NSW];
#pragma unroll
      for (int w = 0; w < NSW / 2; ++w) M = fmaxf(M, fmaxf(ml[w][0], ml[w][2]));
#pragma unroll
      for (int w = 0; w < NSW / 2; ++w) {
        cw[2 * w] = __builtin_amdgcn_exp2f(ml[w][0] - M); cw[2 * w + 1] = __builtin_amdgcn_exp2f(ml[w][2] - M);
        L = fmaf(cw[2 * w], ml[w][1], L); L = fmaf(cw[2 * w + 1], ml[w][3], L);
      }
      f32x4 ov = cw[0] * op[0];
#pragma unroll
      for (int w = 1; w < NSW; ++w) ov += cw[w] * op[w];
      float inv = __builtin_amdgcn_rcpf(L);
      inv = fmaf(fmaf(-L, inv, 1.0f), inv, inv);
      float o[4];
#pragma unroll
      for (int i = 0; i < 4; ++i) o[i] = rbf(ov[i] * inv);
      float e[2][2];
#pragma unroll
      for (int cb = 0; cb < 2; ++cb) {
        f32x2 ac[4];
#pragma unroll
        for (int i = 0; i < 4; ++i)
#pragma unroll
          for (int c = 0; c < 4; ++c) ac[c] = i == 0 ? o[0] * wf[cb][0][c] : ac[c] + o[i] * wf[cb][i][c];
        float acc[4];
        acc[0] = red32(ac[0][0], ac[2][0]); acc[1] = red32(ac[0][1], ac[2][1]);
        acc[2] = red32(ac[1][0], ac[3][0]); acc[3] = red32(ac[1][1], ac[3][1]);
#pragma unroll
        for (int c = 0; c < 2; ++c) e[cb][c] = row_sum8_odd_even(red16(acc[c], acc[c + 2]));
      }
      const float e0 = (lane & 2) ? e[1][0] : e[0][0], e1 = (lane & 2) ? e[1][1] : e[0][1];
      if ((lane & 12) == 0)
        ST16((u32x4{f24(e0, tlo), thi, f24(e1, tlo), thi}),
             WS_H2 + (((b * 32 + jc * 8 + 2 * pw + ((lane >> 1) & 1)) * 32 + kvh * 4 + jh) * 16 + 8 * (lane & 1) + 4 * (lane >> 5) + 2 * ((lane >> 4) & 1)) * 8);
    }
  }
}

}

extern "C" void kernel_launch(void* const* d_in, const int* in_sizes, int n_in, void* d_out, int out_size, void* d_ws,
                              size_t ws_size, hipStream_t stream) {
  Params p;
  p.x = (const unsigned short*)d_in[0]; p.k = (const unsigned short*)d_in[1]; p.v = (const unsigned short*)d_in[2];
  p.wq = (const unsigned short*)d_in[3]; p.wo = (const unsigned short*)d_in[4];
  p.w1 = (const unsigned short*)d_in[6]; p.w2 = (const unsigned short*)d_in[7];
  p.out = (unsigned short*)d_out; p.ws = (unsigned char*)d_ws;
  hipLaunchKernelGGL(fused_decode, dim3(256), dim3(NTHR), 0, stream, p);
}
```
